```python
import torch

def _softmax(x, axis=-1):
    return torch.softmax(x, dim=axis)
def _gelu(x):
    return torch.nn.functional.gelu(x, approximate="tanh")
def _silu(x):
    return torch.nn.functional.silu(x)
def _sigmoid(x):
    return torch.sigmoid(x)
def _layernorm(x, axis=-1, eps=1e-5):
    m = x.mean(dim=axis, keepdim=True)
    v = x.var(dim=axis, keepdim=True, unbiased=False)
    return (x - m) / torch.sqrt(v + eps)
def _scatter_add(tgt, idx, src, axis=0):
    return tgt.clone().index_add_(axis, idx, src)
def _segment_sum(src, seg, n):
    out = torch.zeros((n,) + src.shape[1:], dtype=src.dtype)
    return out.index_add_(0, seg, src)
def _pad(x, pw):
    flat = tuple(v for pair in reversed(pw) for v in pair)
    return torch.nn.functional.pad(x, flat)

kernel_name = 'sequential_preLN__ssm_conv_s4__glu_gated_bilinear__prompt'

def setup_inputs():

    _w = (lambda a: (lambda t: t.to(torch.bfloat16) if t.is_floating_point() else t)(torch.as_tensor(a)))
    def _A_setup():
        B, L, D, N = 1, 2048, 640, 128
        A_re = -(lambda _x: torch.exp(torch.as_tensor(_x)))((lambda *s: torch.randn(*s, dtype=torch.bfloat16) * 6.4)(D, N))
        A_im = torch.pi * (lambda *s: torch.randn(*s, dtype=torch.bfloat16) * 0.05)(D, N)
        B_re = (lambda *s: torch.randn(*s, dtype=torch.bfloat16) * 0.05)(D, N)
        B_im = (lambda *s: torch.randn(*s, dtype=torch.bfloat16) * 0.05)(D, N)
        C_re = (lambda *s: torch.randn(*s, dtype=torch.bfloat16) * 0.05)(D, N)
        C_im = (lambda *s: torch.randn(*s, dtype=torch.bfloat16) * 0.05)(D, N)
        log_dt = (lambda *s: torch.randn(*s, dtype=torch.bfloat16) * 0.05)(D) * 0.5 - 4.0
        Dskip = (lambda *s: torch.randn(*s, dtype=torch.bfloat16) * 0.05)(D)
        return {
            "u": _w((lambda *s: torch.randn(*s, dtype=torch.bfloat16) * 0.05)(B, L, D)),
            "A_re": _w(A_re),
            "A_im": _w(A_im),
            "B_re": _w(B_re),
            "B_im": _w(B_im),
            "C_re": _w(C_re),
            "C_im": _w(C_im),
            "log_dt": _w(log_dt),
            "Dskip": _w(Dskip),
        }
    _A_in = _A_setup()

    def _B_setup():
        B, S, D, F = 1, 2048, 640, 1728
        return {
            "x": _w((lambda *s: torch.randn(*s, dtype=torch.bfloat16) * 0.05)(B, S, D)),
            "w_gate": _w((lambda *s: torch.randn(*s, dtype=torch.bfloat16) * 0.4)(D, F) * (D ** -0.5)),
            "w_up": _w((lambda *s: torch.randn(*s, dtype=torch.bfloat16) * 0.4)(D, F) * (D ** -0.5)),
            "w_down": _w((lambda *s: torch.randn(*s, dtype=torch.bfloat16) * 0.4)(F, D) * (F ** -0.5)),
        }
    _B_in = _B_setup()

    return {
        "A_u": _A_in["u"],
        "A_A_re": _A_in["A_re"],
        "A_A_im": _A_in["A_im"],
        "A_B_re": _A_in["B_re"],
        "A_B_im": _A_in["B_im"],
        "A_C_re": _A_in["C_re"],
        "A_C_im": _A_in["C_im"],
        "A_log_dt": _A_in["log_dt"],
        "A_Dskip": _A_in["Dskip"],
        "B_x": _B_in["x"],
        "B_w_gate": _B_in["w_gate"],
        "B_w_up": _B_in["w_up"],
        "B_w_down": _B_in["w_down"],
    }

def reference(A_u, A_A_re, A_A_im, A_B_re, A_B_im, A_C_re, A_C_im, A_log_dt, A_Dskip, B_x, B_w_gate, B_w_up, B_w_down):
    def _A_ref(u, A_re, A_im, B_re, B_im, C_re, C_im, log_dt, Dskip):
        Bsz, L, D = u.shape
        dt = torch.exp(log_dt)[:, None]
        A = A_re + 1j * A_im
        Bc = B_re + 1j * B_im
        Cc = C_re + 1j * C_im
        logdA = dt * A
        dB = dt * Bc
        l = (lambda x, dt: x.to(dt))(torch.arange(L), A_re.dtype)
        P = torch.exp(logdA[:, :, None] * l[None, None, :])
        K = torch.einsum("dn,dnl,dn->dl", Cc, P, dB).real
        L2 = 2 * L
        Kp = _pad(K, ((0, 0), (0, L)))
        up = (lambda x, *d: x.permute(*d))(u, 0, 2, 1)
        up = _pad(up, ((0, 0), (0, 0), (0, L)))
        Kf = (lambda x, axes=(-2,-1): torch.fft.rfft2(x.float(), dim=axes))(Kp[:, None, :], axes=(-2, -1))
        Uf = (lambda x, axes=(-2,-1): torch.fft.rfft2(x.float(), dim=axes))(up[:, :, None, :], axes=(-2, -1))
        Yf = Uf * Kf[None, :, :, :]
        y = (lambda x, s, axes=(-2,-1): torch.fft.irfft2(x, s=s, dim=axes).to(torch.bfloat16))(Yf, (1, L2), axes=(-2, -1))[:, :, 0, :L]
        y = (lambda x, *d: x.permute(*d))(y, 0, 2, 1) + u * Dskip[None, None, :]
        return y

    def _B_ref(x, w_gate, w_up, w_down):
        g = torch.einsum("bsd,df->bsf", x, w_gate)
        u = torch.einsum("bsd,df->bsf", x, w_up)
        h = g * u
        return torch.einsum("bsf,fd->bsd", h, w_down)

    x = A_u
    h = x + _A_ref(_layernorm(x, axis=-1), A_A_re, A_A_im, A_B_re, A_B_im, A_C_re, A_C_im, A_log_dt, A_Dskip)
    h2 = h + _B_ref(_layernorm(h, axis=-1), B_w_gate, B_w_up, B_w_down)
    return h2
```

```cpp
#include <hip/hip_runtime.h>
#include <stdint.h>

typedef unsigned short u16;
typedef unsigned int u32;
typedef unsigned long long u64;
typedef float f32x2 __attribute__((ext_vector_type(2)));
typedef float f32x4 __attribute__((ext_vector_type(4)));
typedef __bf16 bf16x2 __attribute__((ext_vector_type(2)));
typedef __bf16 bf16x4 __attribute__((ext_vector_type(4)));
typedef __bf16 bf16x8 __attribute__((ext_vector_type(8)));
typedef u32 u32x2 __attribute__((ext_vector_type(2)));
typedef u32 u32x4 __attribute__((ext_vector_type(4)));
#define LAS __attribute__((address_space(3)))
#define GAS __attribute__((address_space(1)))
#define DI __device__ __forceinline__
#define RLX_AGENT __ATOMIC_RELAXED, __HIP_MEMORY_SCOPE_AGENT

constexpr int L_ = 2048, D_ = 640, NS_ = 128, F_ = 1728;

DI float bflo(u32 v) { return __uint_as_float(v << 16); }
DI float bfhi(u32 v) { return __uint_as_float(v & 0xffff0000u); }
DI float bf2f(u16 v) { return __uint_as_float((u32)v << 16); }
DI u32 cvtpk(float lo, float hi) {
  f32x2 v = {lo, hi};
  bf16x2 b = __builtin_convertvector(v, bf16x2);
  return __builtin_bit_cast(u32, b);
}
DI float rbf(float x) { return bflo(cvtpk(x, x)); }
DI u32 rfl(u32 x) { return __builtin_amdgcn_readfirstlane(x); }

template <int CTRL>
DI float dpp_add(float v) {
  int t = __builtin_amdgcn_update_dpp(0, __float_as_int(v), CTRL, 0xf, 0xf, true);
  return v + __int_as_float(t);
}
DI float wave_sum(float v) {
  v = dpp_add<0x128>(v);
  v = dpp_add<0x124>(v);
  v = dpp_add<0x122>(v);
  v = dpp_add<0x121>(v);
  int i = __float_as_int(v);
  float a = __int_as_float(__builtin_amdgcn_readlane(i, 0)), b = __int_as_float(__builtin_amdgcn_readlane(i, 16));
  float c = __int_as_float(__builtin_amdgcn_readlane(i, 32)), d = __int_as_float(__builtin_amdgcn_readlane(i, 48));
  return (a + b) + (c + d);
}

DI f32x2 cmul(f32x2 a, f32x2 b) {
  f32x2 r;
  asm("v_pk_mul_f32 %0, %1, %2 op_sel:[0,0] op_sel_hi:[0,1]" : "=v"(r) : "v"(a), "v"(b));
  asm("v_pk_fma_f32 %0, %1, %2, %0 op_sel:[1,1,0] op_sel_hi:[1,0,1] neg_lo:[0,1,0]" : "+v"(r) : "v"(a), "v"(b));
  return r;
}
DI f32x2 zpow(float ar, float ai, float p) {
  const float er = __builtin_amdgcn_exp2f(ar * p * 1.44269504088896341f);
  const float t = ai * p;
  const float CH = 0.15915494309189535f, CL = 6.4206383e-09f;
  const float hi = t * CH;
  float lo = __builtin_fmaf(t, CH, -hi);
  lo = __builtin_fmaf(t, CL, lo);
  const float fr = __builtin_amdgcn_fractf(hi) + lo;
  f32x2 r = {er * __builtin_amdgcn_cosf(fr), er * __builtin_amdgcn_sinf(fr)};
  return r;
}

DI bf16x8 cat8(bf16x4 a, bf16x4 b) { return __builtin_shufflevector(a, b, 0, 1, 2, 3, 4, 5, 6, 7); }
DI bf16x4 trread(LAS char* p) { return __builtin_amdgcn_ds_read_tr16_b64_v4bf16((LAS bf16x4*)p); }
DI f32x4 mfma16(bf16x8 a, bf16x8 b, f32x4 c) { return __builtin_amdgcn_mfma_f32_16x16x32_bf16(a, b, c, 0, 0, 0); }
DI void lds_add(LAS float* p, float v) { __hip_atomic_fetch_add(p, v, __ATOMIC_RELAXED, __HIP_MEMORY_SCOPE_WORKGROUP); }

DI u32 ln_pack(float mean, float var) {
  const float vb = rbf(var);
  const float t = rbf(vb + 1e-5f);
  const float s = __builtin_sqrtf(t);
  return cvtpk(mean, s);
}

constexpr int KC_COPY = 4160;
constexpr int KC_CH = 4 * KC_COPY;
constexpr int K12_KC = 0;
constexpr int K12_US = 49920;
constexpr int K12_XS = K12_US + 3 * 4608;
constexpr int K12_KD = K12_XS + 3 * 4096;
constexpr int KD_CH = 684, KD0 = 0, KD1 = 288, KD2 = 420, KD3 = 552;
constexpr int K12_SCR = K12_KD + 3 * KD_CH * 4;
constexpr int K12_LDS = K12_SCR + 6 * 8192;
constexpr int NCONV = 214;

__global__ __launch_bounds__(512, 2) void k12(const u16* __restrict__ x, const u16* __restrict__ Are, const u16* __restrict__ Aim,
                                              const u16* __restrict__ Bre, const u16* __restrict__ Bim, const u16* __restrict__ Cre,
                                              const u16* __restrict__ Cim, const u16* __restrict__ logdt, const u16* __restrict__ Dsk,
                                              const u16* __restrict__ wg, const u16* __restrict__ wu, const u16* __restrict__ wd,
                                              u16* __restrict__ hT, float* __restrict__ cs, u64* gran, u32* epoch) {
  extern __shared__ __attribute__((aligned(16))) char smem[];
  LAS char* sm = (LAS char*)smem;
  const int tid = threadIdx.x, lane = tid & 63;
  const int wid = rfl(tid >> 6);
  const int bid = blockIdx.x, xcd = bid & 7, slot = bid >> 3;
  const int wq = xcd * 27 + slot;
  const bool is_conv = slot < 27 && wq < NCONV;
  const int c0 = min(3 * wq, D_ - 3);

  const u32 tag = __hip_atomic_load((GAS u32*)epoch, RLX_AGENT) + 1u;

  const int tok = bid * 8 + wid;
  u32 xr[5];
  {
    const u32* p = (const u32*)(x + tok * D_);
#pragma unroll
    for (int i = 0; i < 5; ++i) xr[i] = p[lane + 64 * i];
  }
  u16 pa[6];
  u16 ldt = 0, dsk[3];
  u32x2 xv[4];
  const int e0 = min((c0 & ~1), D_ - 4), sh = c0 - e0;
  if (is_conv) {
    if (wid < 6) {
      const int o = (c0 + (wid >> 1)) * NS_ + (wid & 1) * 64 + lane;
      pa[0] = Are[o]; pa[1] = Aim[o]; pa[2] = Bre[o]; pa[3] = Bim[o]; pa[4] = Cre[o]; pa[5] = Cim[o];
      ldt = logdt[c0 + (wid >> 1)];
    }
#pragma unroll
    for (int k = 0; k < 4; ++k) xv[k] = *(const u32x2*)(x + (4 * tid + k) * D_ + e0);
#pragma unroll
    for (int k = 0; k < 3; ++k) dsk[k] = Dsk[c0 + k];
    for (int i = tid; i < 3 * KD_CH; i += 512) *(LAS float*)(sm + K12_KD + 4 * i) = 0.f;
    if (tid < 3 * 128) *(LAS u32*)(sm + K12_US + (tid >> 7) * 4608 + (tid & 127) * 4) = 0u;
  }

  {
    float f[10], s = 0.f;
#pragma unroll
    for (int i = 0; i < 5; ++i) { f[2 * i] = bflo(xr[i]); f[2 * i + 1] = bfhi(xr[i]); s += f[2 * i] + f[2 * i + 1]; }
    const float mean = wave_sum(s) * (1.f / D_);
    float q = 0.f;
#pragma unroll
    for (int i = 0; i < 10; ++i) { const float d = f[i] - mean; q = __builtin_fmaf(d, d, q); }
    const float var = wave_sum(q) * (1.f / D_);
    const u32 st = ln_pack(mean, var);
    if (lane == 0) __hip_atomic_store((GAS u64*)(gran + tok), ((u64)tag << 32) | st, RLX_AGENT);
  }

  if (!is_conv) {
    if (slot < 27) return;
    const int sp = slot - 27, fg = xcd >> 1;
    const int ci = tid % 22, rg = tid / 22, ch = 22 * sp + ci;
    float a[8] = {0.f, 0.f, 0.f, 0.f, 0.f, 0.f, 0.f, 0.f};
    if (rg < 23 && ch < 108) {
      const u16* w = (ch < 54 ? wg + 8 * ch : wu + 8 * (ch - 54)) + 432 * fg;
#pragma unroll 7
      for (int d = rg; d < D_; d += 23) {
        const u32x4 v = *(const u32x4*)(w + d * F_);
#pragma unroll
        for (int j = 0; j < 4; ++j) { a[2 * j] += bflo(v[j]); a[2 * j + 1] += bfhi(v[j]); }
      }
    }
    {
      const int g = (sp * 8 + xcd) * 512 + tid;
      u32 acc = 0;
#pragma unroll
      for (int i = 0; i < 7; ++i) {
        const int p = g + i * 20480;
        if (p < F_ * D_ / 8) acc ^= ((const u32x4*)wd)[p][0];
      }
      asm volatile("" ::"v"(acc));
    }
    LAS float* red = (LAS float*)sm;
    if (rg < 23) {
#pragma unroll
      for (int j = 0; j < 8; ++j) red[rg * 176 + ci * 8 + j] = a[j];
    }
    __syncthreads();
    if (tid < 176) {
      const int c2 = 22 * sp + (tid >> 3);
      if (c2 < 108) {
        float s = 0.f;
        for (int r = 0; r < 23; ++r) s += red[r * 176 + tid];
        const int f = 432 * fg + 8 * (c2 < 54 ? c2 : c2 - 54) + (tid & 7);
        cs[(c2 < 54 ? 0 : F_) + f] = s;
      }
    }
    return;
  }

  __syncthreads();

  if (wid < 6) {
    const int ch = wid >> 1;
    const float dt = rbf(expf(bf2f(ldt)));
    const float ar = dt * bf2f(pa[0]), ai = dt * bf2f(pa[1]);
    const float br = dt * bf2f(pa[2]), bi = dt * bf2f(pa[3]);
    const float cr = bf2f(pa[4]), ci = bf2f(pa[5]);
    const f32x2 c = {cr * br - ci * bi, cr * bi + ci * br};
    const f32x2 z1 = zpow(ar, ai, 1.f), z16 = zpow(ar, ai, 16.f), z256 = zpow(ar, ai, 256.f);
    LAS char* s0 = sm + K12_SCR + wid * 8192;
    LAS char* s1 = s0 + 4096;
    const int sw = ((lane >> 2) & 1) * 16;
    LAS char* wre0 = s0 + lane * 32;
    {
      f32x2 v[16];
      v[0] = c;
#pragma unroll
      for (int i = 1; i < 16; ++i) v[i] = cmul(v[i - 1], z16);
      u32x4 a, b, d, e;
#pragma unroll
      for (int i = 0; i < 4; ++i) {
        a[i] = cvtpk(v[2 * i].x, v[2 * i + 1].x); b[i] = cvtpk(v[8 + 2 * i].x, v[9 + 2 * i].x);
        d[i] = cvtpk(v[2 * i].y, v[2 * i + 1].y); e[i] = cvtpk(v[8 + 2 * i].y, v[9 + 2 * i].y);
      }
      *(LAS u32x4*)(wre0 + sw) = a; *(LAS u32x4*)(wre0 + (sw ^ 16)) = b;
      *(LAS u32x4*)(wre0 + 2048 + sw) = d; *(LAS u32x4*)(wre0 + 2048 + (sw ^ 16)) = e;
    }
    f32x2 P[16];
    P[0] = (f32x2){1.f, 0.f}; P[1] = z1;
#pragma unroll
    for (int i = 2; i < 16; ++i) P[i] = (i & 1) ? cmul(P[i - 1], z1) : cmul(P[i / 2], P[i / 2]);
    const int q = lane >> 4, lo16 = lane & 15;
    const int rdo = (4 * q + (lo16 >> 2)) * 32 + ((((lane & 3) >> 1) ^ (q & 1)) * 16) + (lane & 1) * 8;
    LAS char* wre1 = s1 + lane * 32;
    auto put = [&](const f32x2 (&v)[16]) {
      u32x4 a, b, d, e;
#pragma unroll
      for (int i = 0; i < 4; ++i) {
        a[i] = cvtpk(v[2 * i].x, v[2 * i + 1].x); b[i] = cvtpk(v[8 + 2 * i].x, v[9 + 2 * i].x);
        d[i] = cvtpk(-v[2 * i].y, -v[2 * i + 1].y); e[i] = cvtpk(-v[8 + 2 * i].y, -v[9 + 2 * i].y);
      }
      *(LAS u32x4*)(wre1 + sw) = a; *(LAS u32x4*)(wre1 + (sw ^ 16)) = b;
      *(LAS u32x4*)(wre1 + 2048 + sw) = d; *(LAS u32x4*)(wre1 + 2048 + (sw ^ 16)) = e;
    };
    bf16x8 Lf[4];
    auto mm = [&]() {
      f32x4 acc = {0.f, 0.f, 0.f, 0.f};
#pragma unroll
      for (int s = 0; s < 4; ++s) {
        const bf16x8 hf = cat8(trread(s1 + rdo + s * 1024), trread(s1 + rdo + s * 1024 + 512));
        acc = mfma16(Lf[s], hf, acc);
      }
      return acc;
    };
    LAS float* kd = (LAS float*)(sm + K12_KD) + ch * KD_CH;
    put(P);
#pragma unroll
    for (int s = 0; s < 4; ++s) Lf[s] = cat8(trread(s0 + rdo + s * 1024), trread(s0 + rdo + s * 1024 + 512));
    {
      const f32x4 d = mm();
#pragma unroll
      for (int r = 0; r < 4; ++r) lds_add(kd + KD0 + 16 + 16 * (4 * q + r) + lo16, d[r]);
    }
    f32x2 zb[9];
    zb[1] = z256;
#pragma unroll
    for (int k = 2; k < 9; ++k) zb[k] = (k & 1) ? cmul(zb[k - 1], z256) : cmul(zb[k / 2], zb[k / 2]);
    {
      f32x2 v[16];
      v[0] = zb[1];
#pragma unroll
      for (int i = 1; i < 8; ++i) v[i] = cmul(zb[1], P[2 * i]);
      v[8] = zb[2]; v[12] = zb[3];
#pragma unroll
      for (int i = 1; i < 4; ++i) { v[8 + i] = cmul(zb[2], P[4 * i]); v[12 + i] = cmul(zb[3], P[4 * i]); }
      put(v);
      const f32x4 d = mm();
      const int h = lo16;
#pragma unroll
      for (int r = 0; r < 4; ++r) {
        const int lo = 4 * q + r;
        const int idx = h < 8 ? KD1 + 8 * lo + h : KD2 + 64 * ((h - 8) >> 2) + 4 * lo + (h & 3);
        lds_add(kd + idx, d[r]);
      }
      if (q == 0) {
        if (h == 0) { lds_add(kd + KD0 + 16 + 256, d[0]); lds_add(kd + KD0 + 16 + 257, d[0]); }
        if (h == 1) lds_add(kd + KD0 + 16 + 258, d[0]);
        if (h == 2) lds_add(kd + KD0 + 16 + 259, d[0]);
        if (h == 8) { lds_add(kd + KD1 + 128, d[0]); lds_add(kd + KD1 + 129, d[0]); }
        if (h == 9) lds_add(kd + KD1 + 130, d[0]);
      }
    }
    {
      f32x2 v[16];
#pragma unroll
      for (int i = 0; i < 4; ++i) { v[2 * i] = zb[4 + i]; v[2 * i + 1] = cmul(zb[4 + i], P[8]); }
      v[8] = zb[8];
#pragma unroll
      for (int i = 9; i < 16; ++i) v[i] = (f32x2){0.f, 0.f};
      put(v);
      const f32x4 d = mm();
      const int h = lo16;
      if (h < 8) {
#pragma unroll
        for (int r = 0; r < 4; ++r) lds_add(kd + KD3 + 32 * (h >> 1) + 2 * (4 * q + r) + (h & 1), d[r]);
      }
      if (q == 0) {
        if (h == 8) lds_add(kd + KD3 + 128, d[0]);
        if (h == 0) { lds_add(kd + KD2 + 128, d[0]); lds_add(kd + KD2 + 129, d[0]); }
      }
    }
  }

  u32 px[3][2];
  {
    u32 lo[4], hi[4];
#pragma unroll
    for (int k = 0; k < 4; ++k) {
      lo[k] = sh ? __builtin_amdgcn_alignbit(xv[k][1], xv[k][0], 16) : xv[k][0];
      hi[k] = sh ? (xv[k][1] >> 16) : xv[k][1];
    }
#pragma unroll
    for (int p = 0; p < 2; ++p) {
      px[0][p] = __builtin_amdgcn_perm(lo[2 * p + 1], lo[2 * p], 0x05040100u);
      px[1][p] = __builtin_amdgcn_perm(lo[2 * p + 1], lo[2 * p], 0x07060302u);
      px[2][p] = __builtin_amdgcn_perm(hi[2 * p + 1], hi[2 * p], 0x05040100u);
    }
#pragma unroll
    for (int c = 0; c < 3; ++c) *(LAS u32x2*)(sm + K12_XS + c * 4096 + tid * 8) = (u32x2){px[c][0], px[c][1]};
  }
  u64 g[4];
#pragma unroll
  for (int k = 0; k < 4; ++k) g[k] = __hip_atomic_load((GAS u64*)(gran + 4 * tid + k), RLX_AGENT);

  __syncthreads();

  {
#pragma unroll
    for (int rnd = 0; rnd < 2; ++rnd) {
      const int wt = rnd * 8 + wid;
      float s[11];
      int ch = 0, M = 0;
      bool act = true;
      if (wt < 6) {
        ch = wt >> 1;
        const int t = (wt & 1) * 64 + lane;
        M = 1032 + 8 * t;
        LAS float* kd = (LAS float*)(sm + K12_KD) + ch * KD_CH + KD3 + t;
        const float b = kd[0], a = kd[1];
#pragma unroll
        for (int i = 0; i < 7; ++i) s[i] = a;
        s[7] = (t & 1) ? a : b;
        s[8] = s[9] = s[10] = b;
      } else if (wt < 9) {
        ch = wt - 6;
        M = 520 + 8 * lane;
        LAS float* kd = (LAS float*)(sm + K12_KD) + ch * KD_CH + KD2 + 2 * lane;
        const f32x2 u = *(LAS f32x2*)kd, v = *(LAS f32x2*)(kd + 2);
        s[0] = v.y; s[1] = s[2] = s[3] = s[4] = s[5] = v.x; s[6] = s[7] = s[8] = u.y; s[9] = s[10] = u.x;
      } else if (wt < 11) {
        const int T = (wt - 9) * 64 + lane;
        act = T < 96;
        ch = min(T >> 5, 2);
        const int t = T & 31;
        M = 264 + 8 * t;
        LAS float* kd = (LAS float*)(sm + K12_KD) + ch * KD_CH + KD1 + 4 * t;
        const f32x4 u = *(LAS f32x4*)kd, v = *(LAS f32x4*)(kd + 4);
        s[0] = v[2]; s[1] = v[1]; s[2] = s[3] = s[4] = v[0]; s[5] = u[3]; s[6] = s[7] = s[8] = u[2]; s[9] = u[1]; s[10] = u[0];
      } else if (wt < 13) {
        const int T = (wt - 11) * 64 + lane;
        act = T < 102;
        ch = min(T / 34, 2);
        const int t = min(T - ch * 34, 33);
        M = -8 + 8 * t;
        LAS float* kd = (LAS float*)(sm + K12_KD) + ch * KD_CH + KD0 + 8 * t;
        const f32x4 u = *(LAS f32x4*)kd, v = *(LAS f32x4*)(kd + 4), w = *(LAS f32x4*)(kd + 8);
        s[0] = w[3]; s[1] = w[2]; s[2] = w[1]; s[3] = w[0]; s[4] = v[3]; s[5] = v[2]; s[6] = v[1]; s[7] = v[0];
        s[8] = u[3]; s[9] = u[2]; s[10] = u[1];
      } else {
        act = false;
#pragma unroll
        for (int i = 0; i < 11; ++i) s[i] = 0.f;
      }
      if (act) {
        u32 E[5], O[5];
#pragma unroll
        for (int k = 0; k < 5; ++k) { E[k] = cvtpk(s[2 * k], s[2 * k + 1]); O[k] = cvtpk(s[2 * k + 1], s[2 * k + 2]); }
        LAS char* dst = sm + K12_KC + ch * KC_CH + 2 * (2048 - M);
        *(LAS u32x4*)(dst + 3 * KC_COPY) = (u32x4){E[0], E[1], E[2], E[3]};
        *(LAS u32x4*)(dst + 2 * KC_COPY) = (u32x4){O[0], O[1], O[2], O[3]};
        *(LAS u32x4*)(dst + 1 * KC_COPY) = (u32x4){E[1], E[2], E[3], E[4]};
        *(LAS u32x4*)(dst + 0 * KC_COPY) = (u32x4){O[1], O[2], O[3], O[4]};
      }
    }
  }

  {
    u32 spins = 0;
    for (;;) {
      bool ok = true;
#pragma unroll
      for (int k = 0; k < 4; ++k) ok &= (u32)(g[k] >> 32) == tag;
      if (__all(ok)) break;
      if (++spins > (1u << 14)) {
#pragma unroll 1
        for (int k = 0; k < 4; ++k) {
          if ((u32)(g[k] >> 32) != tag) {
            const u16* p = x + (4 * tid + k) * D_;
            float s = 0.f;
            for (int i = 0; i < D_; ++i) s += bf2f(p[i]);
            const float mean = s * (1.f / D_);
            float qq = 0.f;
            for (int i = 0; i < D_; ++i) { const float d = bf2f(p[i]) - mean; qq = __builtin_fmaf(d, d, qq); }
            const u64 v = ((u64)tag << 32) | ln_pack(mean, qq * (1.f / D_));
            if (k == 0) g[0] = v; else if (k == 1) g[1] = v; else if (k == 2) g[2] = v; else g[3] = v;
          }
        }
        break;
      }
      __builtin_amdgcn_s_sleep(1);
#pragma unroll
      for (int k = 0; k < 4; ++k) g[k] = __hip_atomic_load((GAS u64*)(gran + 4 * tid + k), RLX_AGENT);
    }
    float m[4], r[4];
#pragma unroll
    for (int k = 0; k < 4; ++k) { m[k] = bflo((u32)g[k]); r[k] = 1.f / bfhi((u32)g[k]); }
#pragma unroll
    for (int c = 0; c < 3; ++c) {
      u32 o[2];
#pragma unroll
      for (int p = 0; p < 2; ++p) {
        const u32 d = cvtpk(bflo(px[c][p]) - m[2 * p], bfhi(px[c][p]) - m[2 * p + 1]);
        o[p] = cvtpk(bflo(d) * r[2 * p], bfhi(d) * r[2 * p + 1]);
      }
      *(LAS u32x2*)(sm + K12_US + c * 4608 + 512 + tid * 8) = (u32x2){o[0], o[1]};
    }
  }
  __syncthreads();
  if (bid == 0 && tid == 0) __hip_atomic_store((GAS u32*)epoch, tag, RLX_AGENT);

  f32x4 acc[8], sav[8];
  const int b = lane & 15, q = lane >> 4;
  if (wid < 4) {
#pragma unroll
    for (int t = 0; t < 8; ++t) { acc[t] = (f32x4){0.f, 0.f, 0.f, 0.f}; sav[t] = acc[t]; }
    const int kap = b & 3;
    const int kb0 = K12_KC + kap * KC_COPY + 2 * (2048 - 16 - b + 8 * q + kap) - 512 * 7;
    const int ub0 = K12_US + 480 + 32 * b + 16 * q;
    const int n1 = wid == 1 ? 2 : (wid == 2 ? 4 : 6);
#pragma unroll 1
    for (int un = 0; un < 6; ++un) {
      const int U = 6 * wid + un, ch = U >> 3, r = (U & 7) * 2;
      if (un == n1) {
#pragma unroll
        for (int t = 0; t < 8; ++t) { sav[t] = acc[t]; acc[t] = (f32x4){0.f, 0.f, 0.f, 0.f}; }
      }
      LAS char* kb = sm + kb0 + ch * KC_CH - 32 * r;
      LAS char* ub = sm + ub0 + ch * 4608 - 32 * r;
      bf16x8 A[8], B[8];
#pragma unroll
      for (int i = 0; i < 8; ++i) {
        A[i] = cat8(*(LAS bf16x4*)(kb + 512 * (7 - i)), *(LAS bf16x4*)(kb + 512 * (7 - i) + 8));
        B[i] = *(LAS bf16x8*)(ub + 512 * i);
      }
#pragma unroll
      for (int t = 0; t < 8; ++t)
#pragma unroll
        for (int i = 0; i <= t; ++i) acc[t] = mfma16(A[i], B[t - i], acc[t]);
    }
    LAS char* pp = sm + K12_SCR + lane * 16;
    if (wid == 1 || wid == 2) {
#pragma unroll
      for (int t = 0; t < 8; ++t) *(LAS f32x4*)(pp + (wid - 1) * 8192 + t * 1024) = sav[t];
    }
    if (wid == 2) {
#pragma unroll
      for (int t = 0; t < 8; ++t) *(LAS f32x4*)(pp + 2 * 8192 + t * 1024) = acc[t];
    }
  }
  __syncthreads();
  if (wid < 4 && wid != 2) {
    const int ch = wid == 3 ? 2 : wid;
    const float dk = bf2f(ch == 0 ? dsk[0] : (ch == 1 ? dsk[1] : dsk[2]));
    LAS char* pp = sm + K12_SCR + ch * 8192 + lane * 16;
    u16* dst = hT + (c0 + ch) * L_ + 16 * b + 4 * q;
#pragma unroll
    for (int t = 0; t < 8; ++t) {
      const f32x4 y = acc[t] + *(LAS f32x4*)(pp + t * 1024);
      const u32x2 xx = *(LAS u32x2*)(sm + K12_XS + ch * 4096 + 2 * (256 * t + 16 * b + 4 * q));
      const u32x2 uu = *(LAS u32x2*)(sm + K12_US + ch * 4608 + 512 + 2 * (256 * t + 16 * b + 4 * q));
      const float h0 = bflo(xx[0]) + __builtin_fmaf(bflo(uu[0]), dk, y[0]);
      const float h1 = bfhi(xx[0]) + __builtin_fmaf(bfhi(uu[0]), dk, y[1]);
      const float h2 = bflo(xx[1]) + __builtin_fmaf(bflo(uu[1]), dk, y[2]);
      const float h3 = bfhi(xx[1]) + __builtin_fmaf(bfhi(uu[1]), dk, y[3]);
      *(u32x2*)(dst + 256 * t) = (u32x2){cvtpk(h0, h1), cvtpk(h2, h3)};
    }
  }
}

typedef __amdgpu_buffer_rsrc_t srd_t;
DI srd_t mk_srd(const void* p, u32 bytes) { return __builtin_amdgcn_make_buffer_rsrc((void*)p, (short)0, (int)bytes, 0x00020000); }
DI void dma16(u32 lds_addr, u32 voff, srd_t srd, u32 soff) {
  asm volatile("s_mov_b32 m0, %0\n\ts_nop 0\n\tbuffer_load_dwordx4 %1, %2, %3 offen lds" ::"s"(lds_addr), "v"(voff), "s"(srd), "s"(soff) : "memory");
}
#define WAIT_V(n) asm volatile("s_waitcnt vmcnt(" #n ")" ::: "memory")
#define WAIT_L0() asm volatile("s_waitcnt lgkmcnt(0)" ::: "memory")
#define BAR() __builtin_amdgcn_s_barrier()

constexpr int G1_U = 14352, G1_H = 28928, G1_STAGE = 45312, G1_ST = 3 * G1_STAGE;
constexpr int G1_LDS = G1_ST + 128 * 8;

__global__ __launch_bounds__(256, 1) void gemm1(const u16* __restrict__ hT, const u16* __restrict__ wg, const u16* __restrict__ wu,
                                                const float* __restrict__ cs, u16* __restrict__ H) {
  extern __shared__ __attribute__((aligned(16))) char smem[];
  LAS char* sm = (LAS char*)smem;
  const u32 lds0 = (u32)(size_t)sm;
  const int tid = threadIdx.x, lane = tid & 63;
  const int w = rfl(tid >> 6);
  const int bid = blockIdx.x, xcd = bid & 7, jj = bid >> 3;
  const int mt = (xcd & 1) * 8 + (jj & 7), fs = (xcd >> 1) * 4 + (jj >> 3);
  const int f0 = 8 * ((27 * fs) >> 1), m0 = mt * 128;
  const int mh = w >> 1, nh = w & 1;

  const srd_t srdW = mk_srd((w < 2 ? wg : wu) + f0, (u32)(D_ * F_ - f0) * 2u);
  const srd_t srdH = mk_srd(hT + m0, (u32)(D_ * L_ - m0) * 2u);
  u32 voW[7], voH[4];
#pragma unroll
  for (int i = 0; i < 7; ++i) {
    const int S = 64 * (7 * (w & 1) + i) + lane, rho = S / 14, sg = S - 14 * rho;
    voW[i] = rho * (F_ * 2) + sg * 16;
  }
#pragma unroll
  for (int i = 0; i < 4; ++i) {
    const int rho = 4 * (4 * w + i) + (lane >> 4);
    voH[i] = rho * (L_ * 2) + (((lane & 15) ^ (2 * (rho & 7))) * 16);
  }
  const u32 ldW = lds0 + (w < 2 ? 0 : G1_U) + 7168 * (w & 1), ldH = lds0 + G1_H + 4096 * w;
  auto stage = [&](int kt, int buf) {
    const u32 so = buf * G1_STAGE;
#pragma unroll
    for (int i = 0; i < 7; ++i) dma16(ldW + so + 1024 * i, voW[i], srdW, (u32)kt * (64 * F_ * 2));
#pragma unroll
    for (int i = 0; i < 4; ++i) dma16(ldH + so + 1024 * i, voH[i], srdH, (u32)kt * (64 * L_ * 2));
  };
  stage(0, 0);
  stage(1, 1);

  const int q = lane >> 4, qq = (lane & 15) >> 2, p = lane & 3;
  const int rdW = (p & 1) * G1_U + (4 * q + qq) * 224 + 8 * (p >> 1) + 7 * nh * 16;
  int rdH[4];
#pragma unroll
  for (int t = 0; t < 4; ++t) {
    const int tt = 4 * mh + ((t + 2 * nh) & 3);
    const int X = 2 * (4 * (q & 1) + qq);
    rdH[t] = G1_H + (4 * q + qq) * 256 + (((2 * tt) ^ X) + (p >> 1)) * 16 + 8 * (p & 1);
  }
  f32x4 acc[4][7];
#pragma unroll
  for (int t = 0; t < 4; ++t)
#pragma unroll
    for (int j = 0; j < 7; ++j) acc[t][j] = (f32x4){0.f, 0.f, 0.f, 0.f};
  float s1[2] = {0.f, 0.f}, s2[2] = {0.f, 0.f};
  const bf16x2 ones = {(__bf16)1.0f, (__bf16)1.0f};

#pragma unroll 1
  for (int kt = 0; kt < 10; ++kt) {
    if (kt < 9) WAIT_V(11); else WAIT_V(0);
    WAIT_L0();
    BAR();
    const int buf = kt % 3;
    if (kt + 2 < 10) stage(kt + 2, (kt + 2) % 3);
    LAS char* sb = sm + buf * G1_STAGE;
#pragma unroll
    for (int ks = 0; ks < 2; ++ks) {
      bf16x8 Wf[7], Hf[4];
#pragma unroll
      for (int t = 0; t < 4; ++t) Hf[t] = cat8(trread(sb + rdH[t] + ks * 8192), trread(sb + rdH[t] + ks * 8192 + 4096));
#pragma unroll
      for (int j = 0; j < 7; ++j) Wf[j] = cat8(trread(sb + rdW + ks * 7168 + j * 16), trread(sb + rdW + ks * 7168 + 3584 + j * 16));
#pragma unroll
      for (int t = 0; t < 2; ++t) {
        const u32x4 hv = __builtin_bit_cast(u32x4, Hf[t]);
#pragma unroll
        for (int i = 0; i < 4; ++i) {
          const u32 e = hv[i];
          const bf16x2 v = __builtin_bit_cast(bf16x2, e);
          s1[t] = __builtin_amdgcn_fdot2_f32_bf16(v, ones, s1[t], false);
          s2[t] = __builtin_amdgcn_fdot2_f32_bf16(v, v, s2[t], false);
        }
      }
#pragma unroll
      for (int j = 0; j < 7; ++j)
#pragma unroll
        for (int t = 0; t < 4; ++t) acc[t][j] = mfma16(Wf[j], Hf[t], acc[t][j]);
    }
  }

  LAS f32x2* st = (LAS f32x2*)(sm + G1_ST);
#pragma unroll
  for (int t = 0; t < 2; ++t) {
    float a = s1[t], b2 = s2[t];
    { auto r = __builtin_amdgcn_permlane16_swap(__float_as_uint(a), __float_as_uint(a), false, false); a = __uint_as_float(r[0]) + __uint_as_float(r[1]); }
    { auto r = __builtin_amdgcn_permlane32_swap(__float_as_uint(a), __float_as_uint(a), false, false); a = __uint_as_float(r[0]) + __uint_as_float(r[1]); }
    { auto r = __builtin_amdgcn_permlane16_swap(__float_as_uint(b2), __float_as_uint(b2), false, false); b2 = __uint_as_float(r[0]) + __uint_as_float(r[1]); }
    { auto r = __builtin_amdgcn_permlane32_swap(__float_as_uint(b2), __float_as_uint(b2), false, false); b2 = __uint_as_float(r[0]) + __uint_as_float(r[1]); }
    const float mean = a * (1.f / D_);
    const float var = fmaxf(b2 * (1.f / D_) - mean * mean, 0.f);
    const u32 pk = ln_pack(mean, var);
    const int tt = 4 * mh + ((t + 2 * nh) & 3);
    if (lane < 16) st[tt * 16 + lane] = (f32x2){bflo(pk), 1.f / bfhi(pk)};
  }
  f32x4 cv[7];
  {
    const float* cp = cs + ((q & 1) ? F_ : 0) + f0 + 56 * nh + 4 * (q >> 1);
#pragma unroll
    for (int j = 0; j < 7; ++j) cv[j] = *(const f32x4*)(cp + 8 * j);
  }
  __syncthreads();
  float mean[4], rr[2];
  {
    float rs[4];
#pragma unroll
    for (int t = 0; t < 4; ++t) {
      const int tt = 4 * mh + ((t + 2 * nh) & 3);
      const f32x2 v = st[tt * 16 + (lane & 15)];
      mean[t] = v.x; rs[t] = v.y;
    }
    rr[0] = (q & 1) ? rs[1] * rs[1] : rs[0] * rs[0];
    rr[1] = (q & 1) ? rs[3] * rs[3] : rs[2] * rs[2];
  }
#pragma unroll
  for (int pr = 0; pr < 2; ++pr) {
    const int tt = 4 * mh + ((2 * pr + (q & 1) + 2 * nh) & 3);
    u16* row = H + (size_t)(m0 + tt * 16 + (lane & 15)) * F_ + f0 + 56 * nh;
    u32 pk[7][2];
#pragma unroll
    for (int j = 0; j < 7; ++j) {
      float pv[4];
#pragma unroll
      for (int r = 0; r < 4; ++r) {
        const float a = __builtin_fmaf(-mean[2 * pr], cv[j][r], acc[2 * pr][j][r]);
        const float b = __builtin_fmaf(-mean[2 * pr + 1], cv[j][r], acc[2 * pr + 1][j][r]);
        auto s = __builtin_amdgcn_permlane16_swap(__float_as_uint(a), __float_as_uint(b), false, false);
        pv[r] = __uint_as_float(s[0]) * __uint_as_float(s[1]) * rr[pr];
      }
      pk[j][0] = cvtpk(pv[0], pv[1]); pk[j][1] = cvtpk(pv[2], pv[3]);
    }
#pragma unroll
    for (int j = 0; j < 6; j += 2) {
      auto s0 = __builtin_amdgcn_permlane32_swap(pk[j][0], pk[j + 1][0], false, false);
      auto s1x = __builtin_amdgcn_permlane32_swap(pk[j][1], pk[j + 1][1], false, false);
      *(u32x4*)(row + 8 * j + 8 * (q >> 1)) = (u32x4){s0[0], s1x[0], s0[1], s1x[1]};
    }
    *(u32x2*)(row + 48 + 4 * (q >> 1)) = (u32x2){pk[6][0], pk[6][1]};
  }
}

constexpr int G2_H = 10240, G2_STAGE = 18432, G2_NST = 6, G2_RES = G2_NST * G2_STAGE;
constexpr int G2_LDS = G2_RES + 10240;

__global__ __launch_bounds__(256, 1) void gemm2(const u16* __restrict__ H, const u16* __restrict__ wd, const u16* __restrict__ hT,
                                                u16* __restrict__ out) {
  extern __shared__ __attribute__((aligned(16))) char smem[];
  LAS char* sm = (LAS char*)smem;
  const u32 lds0 = (u32)(size_t)sm;
  const int tid = threadIdx.x, lane = tid & 63;
  const int w = rfl(tid >> 6);
  const int bid = blockIdx.x, xcd = bid & 7, jj = bid >> 3;
  const int fg = xcd >> 1;
  const int mt = (xcd & 1) * 16 + (fg >> 1) * 8 + (jj & 7), nt = (fg & 1) * 4 + (jj >> 3);
  const int m0 = mt * 64, n0 = nt * 80;

  const bool isW = w < 2;
  const srd_t srd = isW ? mk_srd(wd + n0, (u32)(F_ * D_ - n0) * 2u) : mk_srd(H + (size_t)m0 * F_, (u32)(L_ - m0) * F_ * 2u);
  u32 vo[5];
#pragma unroll
  for (int i = 0; i < 5; ++i) {
    if (isW) {
      const int S = 64 * (5 * w + i) + lane, rho = S / 10, sg = S - 10 * rho;
      const int k = (rho & 32) + 8 * ((rho >> 2) & 3) + 4 * ((rho >> 4) & 1) + (rho & 3);
      vo[i] = k * (D_ * 2) + sg * 16;
    } else {
      const int row = 8 * (4 * (w - 2) + (i & 3)) + (lane >> 3);
      vo[i] = row * (F_ * 2) + (((lane & 7) ^ (row & 7)) * 16);
    }
  }
  const u32 ldb = lds0 + (isW ? 5120 * w : G2_H + 4096 * (w - 2));
  const u32 kstride = isW ? 64 * D_ * 2 : 128;
  auto stage = [&](int kt, int buf) {
    const u32 so = buf * G2_STAGE, ko = (u32)kt * kstride;
#pragma unroll
    for (int i = 0; i < 4; ++i) dma16(ldb + so + 1024 * i, vo[i], srd, ko);
    if (isW) dma16(ldb + so + 4096, vo[4], srd, ko);
  };
  {
    const srd_t srdR = mk_srd(hT + (size_t)n0 * L_ + m0, (u32)((D_ - n0) * L_ - m0) * 2u);
#pragma unroll
    for (int i = 0; i < 3; ++i) {
      const int pc = w + 4 * i;
      if (pc < 10) dma16(lds0 + G2_RES + 1024 * pc, (u32)((8 * pc + (lane >> 3)) * (L_ * 2) + (lane & 7) * 16), srdR, 0u);
    }
  }
#pragma unroll
  for (int s = 0; s < G2_NST - 1; ++s) stage(s, s);

  const int q = lane >> 4, qq = (lane & 15) >> 2, p = lane & 3;
  const int rdW = (4 * q + qq) * 160 + 8 * p;
  const int tokl = 16 * w + (lane & 15);
  const int rdH = G2_H + tokl * 128;
  f32x4 acc[5];
#pragma unroll
  for (int d = 0; d < 5; ++d) acc[d] = (f32x4){0.f, 0.f, 0.f, 0.f};

#pragma unroll 1
  for (int kt = 0; kt < 27; ++kt) {
    const int rem = 26 - kt;
    if (rem >= 4) { if (isW) WAIT_V(20); else WAIT_V(16); }
    else WAIT_V(0);
    WAIT_L0();
    BAR();
    if (kt + G2_NST - 1 < 27) stage(kt + G2_NST - 1, (kt + G2_NST - 1) % G2_NST);
    LAS char* sb = sm + (kt % G2_NST) * G2_STAGE;
#pragma unroll
    for (int ks = 0; ks < 2; ++ks) {
      const bf16x8 hf = *(LAS bf16x8*)(sb + rdH + (((4 * ks + q) ^ (tokl & 7)) * 16));
#pragma unroll
      for (int d = 0; d < 5; ++d) {
        const bf16x8 wf = cat8(trread(sb + rdW + ks * 5120 + d * 32), trread(sb + rdW + ks * 5120 + 2560 + d * 32));
        acc[d] = mfma16(wf, hf, acc[d]);
      }
    }
  }
  u16* orow = out + (size_t)(m0 + tokl) * D_ + n0 + 4 * q;
#pragma unroll
  for (int d = 0; d < 5; ++d) {
    const u32x2 hr = __builtin_bit_cast(u32x2, trread(sm + G2_RES + (16 * d + 4 * q + qq) * 128 + 32 * w + 8 * p));
    const float o0 = bflo(hr[0]) + acc[d][0], o1 = bfhi(hr[0]) + acc[d][1], o2 = bflo(hr[1]) + acc[d][2], o3 = bfhi(hr[1]) + acc[d][3];
    *(u32x2*)(orow + 16 * d) = (u32x2){cvtpk(o0, o1), cvtpk(o2, o3)};
  }
}

constexpr size_t WS_EPOCH = 0, WS_GRAN = 4096, WS_CS = WS_GRAN + 16384, WS_HT = 65536, WS_H = WS_HT + (size_t)D_ * L_ * 2;

extern "C" void kernel_launch(void* const* d_in, const int* in_sizes, int n_in, void* d_out, int out_size, void* d_ws, size_t ws_size,
                              hipStream_t stream) {
  char* ws = (char*)d_ws;
  const u16* x = (const u16*)d_in[0];
  u16* hT = (u16*)(ws + WS_HT);
  u16* H = (u16*)(ws + WS_H);
  float* cs = (float*)(ws + WS_CS);
  (void)hipFuncSetAttribute((const void*)k12, hipFuncAttributeMaxDynamicSharedMemorySize, K12_LDS);
  (void)hipFuncSetAttribute((const void*)gemm1, hipFuncAttributeMaxDynamicSharedMemorySize, G1_LDS);
  (void)hipFuncSetAttribute((const void*)gemm2, hipFuncAttributeMaxDynamicSharedMemorySize, G2_LDS);
  k12<<<256, 512, K12_LDS, stream>>>(x, (const u16*)d_in[1], (const u16*)d_in[2], (const u16*)d_in[3], (const u16*)d_in[4],
                                     (const u16*)d_in[5], (const u16*)d_in[6], (const u16*)d_in[7], (const u16*)d_in[8],
                                     (const u16*)d_in[10], (const u16*)d_in[11], (const u16*)d_in[12], hT, cs, (u64*)(ws + WS_GRAN),
                                     (u32*)(ws + WS_EPOCH));
  gemm1<<<256, 256, G1_LDS, stream>>>(hT, (const u16*)d_in[10], (const u16*)d_in[11], cs, H);
  gemm2<<<256, 256, G2_LDS, stream>>>(H, (const u16*)d_in[12], hT, (u16*)d_out);
}
```

```cpp
#include <hip/hip_runtime.h>
#include <stdint.h>

typedef unsigned short u16;
typedef unsigned int u32;
typedef unsigned long long u64;
typedef float f32x2 __attribute__((ext_vector_type(2)));
typedef float f32x4 __attribute__((ext_vector_type(4)));
typedef __bf16 bf16x2 __attribute__((ext_vector_type(2)));
typedef __bf16 bf16x4 __attribute__((ext_vector_type(4)));
typedef __bf16 bf16x8 __attribute__((ext_vector_type(8)));
typedef u32 u32x2 __attribute__((ext_vector_type(2)));
typedef u32 u32x4 __attribute__((ext_vector_type(4)));
#define LAS __attribute__((address_space(3)))
#define GAS __attribute__((address_space(1)))
#define DI __device__ __forceinline__
#define RLX_AGENT __ATOMIC_RELAXED, __HIP_MEMORY_SCOPE_AGENT

#define STAMP_ARG
#define STAMP_PASS(k)
#define STAMP_DECL()
#define STAMP_FWD
#define STAMP_ARG2
#define STAMP_AT(i)
constexpr int L_ = 2048, D_ = 640, NS_ = 128, F_ = 1728;

DI float bflo(u32 v) { return __uint_as_float(v << 16); }
DI float bfhi(u32 v) { return __uint_as_float(v & 0xffff0000u); }
DI float bf2f(u16 v) { return __uint_as_float((u32)v << 16); }
DI u32 cvtpk(float lo, float hi) {
  f32x2 v = {lo, hi};
  bf16x2 b = __builtin_convertvector(v, bf16x2);
  return __builtin_bit_cast(u32, b);
}
DI float rbf(float x) { return bflo(cvtpk(x, x)); }
DI u32 rfl(u32 x) { return __builtin_amdgcn_readfirstlane(x); }

template <int CTRL>
DI float dpp_add(float v) {
  int t = __builtin_amdgcn_update_dpp(0, __float_as_int(v), CTRL, 0xf, 0xf, true);
  return v + __int_as_float(t);
}
DI f32x2 cmul(f32x2 a, f32x2 b) {
  const f32x2 bs = {-b.y, b.x};
  return __builtin_elementwise_fma((f32x2){a.y, a.y}, bs, (f32x2){a.x, a.x} * b);
}
DI f32x2 zpow(float ar, float ai, float p) {
  const float er = __builtin_amdgcn_exp2f(ar * p * 1.44269504088896341f);
  const float t = ai * p;
  const float CH = 0.15915494309189535f, CL = 6.4206383e-09f;
  const float hi = t * CH;
  float lo = __builtin_fmaf(t, CH, -hi);
  lo = __builtin_fmaf(t, CL, lo);
  const float fr = __builtin_amdgcn_fractf(hi) + lo;
  f32x2 r = {er * __builtin_amdgcn_cosf(fr), er * __builtin_amdgcn_sinf(fr)};
  return r;
}

DI bf16x8 cat8(bf16x4 a, bf16x4 b) { return __builtin_shufflevector(a, b, 0, 1, 2, 3, 4, 5, 6, 7); }
DI bf16x4 trread(LAS char* p) { return __builtin_amdgcn_ds_read_tr16_b64_v4bf16((LAS bf16x4*)p); }
DI f32x4 mfma16(bf16x8 a, bf16x8 b, f32x4 c) { return __builtin_amdgcn_mfma_f32_16x16x32_bf16(a, b, c, 0, 0, 0); }

DI u32 ln_pack(float mean, float var) {
  const float vb = rbf(var);
  const float t = rbf(vb + 1e-5f);
  const float s = __builtin_sqrtf(t);
  return cvtpk(mean, s);
}

typedef __amdgpu_buffer_rsrc_t srd_t;
DI srd_t mk_srd(const void* p, u32 bytes) { return __builtin_amdgcn_make_buffer_rsrc((void*)p, (short)0, (int)bytes, 0x00020000); }
DI void dma16(u32 lds_addr, u32 voff, srd_t srd, u32 soff) {
  asm volatile("s_mov_b32 m0, %0\n\ts_nop 0\n\tbuffer_load_dwordx4 %1, %2, %3 offen lds" ::"s"(lds_addr), "v"(voff), "s"(srd), "s"(soff) : "memory");
}
#define WAIT_V(n) asm volatile("s_waitcnt vmcnt(" #n ")" ::: "memory")
#define WAIT_L0() __builtin_amdgcn_s_waitcnt(0xC07F)
#define BAR() __builtin_amdgcn_s_barrier()
#define PIN() __builtin_amdgcn_sched_barrier(0)
DI void vadd_s(u32& a, u32 sadv) { asm volatile("v_add_u32 %0, %1, %0" : "+v"(a) : "s"(sadv)); }
DI void vshl16(u32& d, u32 x) { asm volatile("v_lshlrev_b32 %0, 16, %1" : "=v"(d) : "v"(x)); }
DI void vhi16(u32& d, u32 x) { asm volatile("v_and_b32 %0, 0xffff0000, %1" : "=v"(d) : "v"(x)); }
DI void vaddf(float& a, u32 x) { asm volatile("v_add_f32 %0, %1, %0" : "+v"(a) : "v"(x)); }
DI void vsq(float& a, u32 x) { asm volatile("v_fmac_f32 %0, %1, %1" : "+v"(a) : "v"(x)); }
#define PACK_AUX 16
#define HT_AUX 16
#define H_AUX 16

constexpr int KC_COPY = 4160;
constexpr int KC_CH = 4 * KC_COPY;
constexpr int K12_KC = 0;
constexpr int K12_US = 49920;
constexpr int K12_XS = K12_US + 3 * 4608;
constexpr int KD_CH = 684, KD0 = 0, KD1 = 288, KD2 = 420, KD3 = 552;
constexpr int K12_KD = K12_XS + 3 * 4096;
constexpr int K12_SCR = K12_KD + 6 * KD_CH * 4;
constexpr int K12_WD = K12_SCR + 6 * 8192;
constexpr int K12_CNT = K12_WD + 10240;
constexpr int K12_LDS = K12_CNT + 16;
constexpr int NCONV = 214;
constexpr int PK_U = 14352, PK_SLAB = 28928;

__global__ __launch_bounds__(512, 2) void k12(const u16* __restrict__ x, const u16* __restrict__ Are, const u16* __restrict__ Aim,
                                              const u16* __restrict__ Bre, const u16* __restrict__ Bim, const u16* __restrict__ Cre,
                                              const u16* __restrict__ Cim, const u16* __restrict__ logdt, const u16* __restrict__ Dsk,
                                              const u16* __restrict__ wg, const u16* __restrict__ wu, const u16* __restrict__ wd,
                                              u16* __restrict__ hT, char* __restrict__ wp1, char* __restrict__ wp2,
                                              float* __restrict__ csp, u64* gran, u32* epoch STAMP_ARG) {
  STAMP_DECL();
  STAMP_AT(0);
  extern __shared__ __attribute__((aligned(16))) char smem[];
  LAS char* sm = (LAS char*)smem;
  const u32 lds0 = (u32)(size_t)sm;
  const int tid = threadIdx.x, lane = tid & 63;
  const int wid = rfl(tid >> 6);
  const int bid = blockIdx.x, xcd = bid & 7, slot = bid >> 3;
  const int wq = xcd * 27 + slot;
  const bool is_conv = slot < 27 && wq < NCONV;
  const int c0 = min(3 * wq, D_ - 3);
  const u32 tag = *(volatile const u32*)epoch + 1u;
  const int q = lane >> 4, l16 = lane & 15;
  const int tok = bid * 8 + 4 * (wid & 1) + q;
  u32x2 xr[10];
  if (wid == 2 || wid == 3) {
    const u32x2* p = (const u32x2*)(x + tok * D_);
#pragma unroll
    for (int i = 0; i < 10; ++i) xr[i] = p[l16 + 16 * i];
  }
  u16 pa[6];
  u16 ldt = 0, dsk[3];
  if (is_conv) {
    if (wid < 6) {
      const int o = (c0 + (wid >> 1)) * NS_ + (wid & 1) * 64 + lane;
      pa[0] = Are[o]; pa[1] = Aim[o]; pa[2] = Bre[o]; pa[3] = Bim[o]; pa[4] = Cre[o]; pa[5] = Cim[o];
      ldt = logdt[c0 + (wid >> 1)];
    }
#pragma unroll
    for (int k = 0; k < 3; ++k) dsk[k] = Dsk[c0 + k];
    if (tid < 96) *(LAS float*)(sm + K12_KD + (tid >> 4) * (KD_CH * 4) + (tid & 15) * 4) = 0.f;
    if (tid >= 128) *(LAS u32*)(sm + K12_US + ((tid - 128) >> 7) * 4608 + (tid & 127) * 4) = 0u;
    if (tid == 96) *(LAS u32*)(sm + K12_CNT) = 0u;
    __syncthreads();
  }
  u32x2 xv[4][4];
  const int e0 = min((c0 & ~1), D_ - 4), sh = c0 - e0;
  if (is_conv && wid >= 6) {
#pragma unroll
    for (int g4 = 0; g4 < 4; ++g4)
#pragma unroll
      for (int k = 0; k < 4; ++k) xv[g4][k] = *(const u32x2*)(x + (4 * (tid - 384 + 128 * g4) + k) * D_ + e0);
  }

  if (wid == 2 || wid == 3) {
    const float piv = bflo(__builtin_amdgcn_update_dpp(0u, xr[0][0], 0x150, 0xf, 0xf, true));
    float s = 0.f, qs = 0.f;
#pragma unroll
    for (int i = 0; i < 10; ++i) {
      const float a = bflo(xr[i][0]) - piv, b = bfhi(xr[i][0]) - piv, c = bflo(xr[i][1]) - piv, d = bfhi(xr[i][1]) - piv;
      s += (a + b) + (c + d);
      qs = __builtin_fmaf(a, a, __builtin_fmaf(b, b, __builtin_fmaf(c, c, __builtin_fmaf(d, d, qs))));
    }
    s = dpp_add<0x128>(s); qs = dpp_add<0x128>(qs);
    s = dpp_add<0x124>(s); qs = dpp_add<0x124>(qs);
    s = dpp_add<0x122>(s); qs = dpp_add<0x122>(qs);
    s = dpp_add<0x121>(s); qs = dpp_add<0x121>(qs);
    const float md = s * (1.f / D_);
    const float var = fmaxf(qs * (1.f / D_) - md * md, 0.f);
    const u32 st = ln_pack(piv + md, var);
    if (l16 == 0) __hip_atomic_store((GAS u64*)(gran + tok), ((u64)tag << 32) | st, RLX_AGENT);
  }
  STAMP_AT(1);

  if (slot >= 27) {
    const int idx = (xcd & 1) * 5 + (slot - 27), fg = xcd >> 1;
    const srd_t sg = mk_srd(wg, D_ * F_ * 2), su = mk_srd(wu, D_ * F_ * 2);
    {
      const int s = idx * 4 + (wid >> 1), fs = 4 * fg + s / 10, kt = s % 10, f0 = 8 * ((27 * fs) >> 1);
      const u32 so = (u32)(kt * 64 * F_ + f0) * 2u, lb = lds0 + (wid >> 1) * PK_SLAB + (wid & 1) * PK_U;
#pragma unroll
      for (int i = 0; i < 14; ++i) {
        const int S = 64 * i + lane, rho = S / 14, sgm = S - 14 * rho;
        dma16(lb + 1024 * i, (u32)(rho * (F_ * 2) + sgm * 16), (wid & 1) ? su : sg, so);
      }
    }
    WAIT_V(0);
    BAR();
    const srd_t sp = mk_srd(wp1, 16 * 10 * 28672);
    const int p = lane & 3, qq = l16 >> 2;
    const int rd = (p & 1) * PK_U + (4 * q + qq) * 224 + 8 * (p >> 1);
    const bf16x2 ones = {(__bf16)1.0f, (__bf16)1.0f};
#pragma unroll 1
    for (int it = 0; it < 7; ++it) {
      const int job = it * 8 + wid;
      const int sl = job / 14, ct = job - 14 * sl;
      const int s = idx * 4 + sl, fs = 4 * fg + s / 10, kt = s % 10;
      LAS char* b = sm + sl * PK_SLAB + rd + ct * 16;
      float cs = 0.f;
#pragma unroll
      for (int ks = 0; ks < 2; ++ks) {
        const bf16x8 wf = cat8(trread(b + ks * 7168), trread(b + ks * 7168 + 3584));
        const u32x4 wv = __builtin_bit_cast(u32x4, wf);
#pragma unroll
        for (int i = 0; i < 4; ++i) { const u32 e = wv[i]; cs = __builtin_amdgcn_fdot2_f32_bf16(__builtin_bit_cast(bf16x2, e), ones, cs, false); }
        __builtin_amdgcn_raw_buffer_store_b128(wv, sp, (u32)((((fs * 10 + kt) * 2 + ks) * 14 + ct) * 1024 + lane * 16), 0, PACK_AUX);
      }
      { auto r = __builtin_amdgcn_permlane16_swap(__float_as_uint(cs), __float_as_uint(cs), false, false); cs = __uint_as_float(r[0]) + __uint_as_float(r[1]); }
      { auto r = __builtin_amdgcn_permlane32_swap(__float_as_uint(cs), __float_as_uint(cs), false, false); cs = __uint_as_float(r[0]) + __uint_as_float(r[1]); }
      if (lane < 16) csp[(fs * 10 + kt) * 224 + ct * 16 + lane] = cs;
    }
    return;
  }

  auto pack_wd = [&]() {
    const int ks = wid - 6;
    const srd_t sd = mk_srd(wd, F_ * D_ * 2);
    const u32 so = (u32)((slot * 64 + ks * 32) * D_ + xcd * 80) * 2u, lb = lds0 + K12_WD + ks * 5120;
#pragma unroll
    for (int i = 0; i < 5; ++i) {
      const int S = 64 * i + lane, rho = S / 10, sgm = S - 10 * rho;
      dma16(lb + 1024 * i, (u32)(rho * (D_ * 2) + sgm * 16), sd, so);
    }
    WAIT_V(0);
    const srd_t sp = mk_srd(wp2, 8 * 27 * 10240);
    const int p = lane & 3, qq = l16 >> 2;
    LAS char* b = sm + K12_WD + ks * 5120 + (8 * q + qq) * 160 + 8 * p;
#pragma unroll
    for (int dt = 0; dt < 5; ++dt) {
      const bf16x8 wf = cat8(trread(b + dt * 32), trread(b + 4 * 160 + dt * 32));
      __builtin_amdgcn_raw_buffer_store_b128(__builtin_bit_cast(u32x4, wf), sp, (u32)((((xcd * 27 + slot) * 2 + ks) * 5 + dt) * 1024 + lane * 16), 0, PACK_AUX);
    }
  };
  if (!is_conv) {
    if (wid >= 6) pack_wd();
    return;
  }

  if (wid >= 6) {
    const int t2 = tid - 384;
#pragma unroll
    for (int g4 = 0; g4 < 4; ++g4) {
      u32 lo[4], hi[4];
#pragma unroll
      for (int k = 0; k < 4; ++k) {
        lo[k] = sh ? __builtin_amdgcn_alignbit(xv[g4][k][1], xv[g4][k][0], 16) : xv[g4][k][0];
        hi[k] = sh ? (xv[g4][k][1] >> 16) : xv[g4][k][1];
      }
      LAS char* dst = sm + K12_XS + (t2 + 128 * g4) * 8;
      *(LAS u32x2*)(dst) = (u32x2){__builtin_amdgcn_perm(lo[1], lo[0], 0x05040100u), __builtin_amdgcn_perm(lo[3], lo[2], 0x05040100u)};
      *(LAS u32x2*)(dst + 4096) = (u32x2){__builtin_amdgcn_perm(lo[1], lo[0], 0x07060302u), __builtin_amdgcn_perm(lo[3], lo[2], 0x07060302u)};
      *(LAS u32x2*)(dst + 8192) = (u32x2){__builtin_amdgcn_perm(hi[1], hi[0], 0x05040100u), __builtin_amdgcn_perm(hi[3], hi[2], 0x05040100u)};
    }
  } else {
    {
      const float dt = rbf(expf(bf2f(ldt)));
      const float ar = dt * bf2f(pa[0]), ai = dt * bf2f(pa[1]);
      const float br = dt * bf2f(pa[2]), bi = dt * bf2f(pa[3]);
      const float cr = bf2f(pa[4]), ci = bf2f(pa[5]);
      const f32x2 c = {cr * br - ci * bi, cr * bi + ci * br};
      const f32x2 z16 = zpow(ar, ai, 16.f), y1 = zpow(ar, -ai, 1.f), y256 = zpow(ar, -ai, 256.f);
      const f32x2 z16s = {-z16.y, z16.x}, y1s = {-y1.y, y1.x}, y256s = {-y256.y, y256.x};
      auto cm = [](f32x2 a, f32x2 b, f32x2 bs) { return __builtin_elementwise_fma((f32x2){a.y, a.y}, bs, (f32x2){a.x, a.x} * b); };
      LAS char* s0 = sm + K12_SCR + wid * 8192;
      LAS char* s1 = s0 + 4096;
      const int sw = ((lane >> 2) & 1) * 16;
      auto put = [&](LAS char* base, const f32x2 (&v)[16], bool both) {
        u32x4 a, b, d, e;
#pragma unroll
        for (int i = 0; i < 4; ++i) {
          a[i] = cvtpk(v[2 * i].x, v[2 * i + 1].x); d[i] = cvtpk(v[2 * i].y, v[2 * i + 1].y);
          if (both || i == 0) { b[i] = cvtpk(v[8 + 2 * i].x, v[9 + 2 * i].x); e[i] = cvtpk(v[8 + 2 * i].y, v[9 + 2 * i].y); }
          else { b[i] = 0u; e[i] = 0u; }
        }
        LAS char* w = base + lane * 32;
        *(LAS u32x4*)(w + sw) = a; *(LAS u32x4*)(w + (sw ^ 16)) = b;
        *(LAS u32x4*)(w + 2048 + sw) = d; *(LAS u32x4*)(w + 2048 + (sw ^ 16)) = e;
      };
      f32x2 Lp[16], P[16], zb[9];
      Lp[0] = c; P[0] = (f32x2){1.f, 0.f}; P[1] = y1; zb[1] = y256;
#pragma unroll
      for (int i = 1; i < 16; ++i) {
        Lp[i] = cm(Lp[i - 1], z16, z16s);
        if (i >= 2) P[i] = cm(P[i - 1], y1, y1s);
        if (i >= 2 && i < 9) zb[i] = cm(zb[i - 1], y256, y256s);
        PIN();
      }
      put(s0, Lp, true);
      put(s1, P, true);
      const int rdo = (4 * q + (l16 >> 2)) * 32 + ((((lane & 3) >> 1) ^ (q & 1)) * 16) + (lane & 1) * 8;
      bf16x8 Lf[4];
#pragma unroll
      for (int s = 0; s < 4; ++s) Lf[s] = cat8(trread(s0 + rdo + s * 1024), trread(s0 + rdo + s * 1024 + 512));
      auto mm = [&]() {
        f32x4 acc = {0.f, 0.f, 0.f, 0.f};
#pragma unroll
        for (int s = 0; s < 4; ++s) {
          const bf16x8 hf = cat8(trread(s1 + rdo + s * 1024), trread(s1 + rdo + s * 1024 + 512));
          acc = mfma16(Lf[s], hf, acc);
        }
        return acc;
      };
      LAS float* kd = (LAS float*)(sm + K12_KD) + ((wid & 1) * 3 + (wid >> 1)) * KD_CH;
      const f32x4 dA = mm();
      f32x2 v[16];
      const f32x2 zb2s = {-zb[2].y, zb[2].x}, zb3s = {-zb[3].y, zb[3].x}, p8s = {-P[8].y, P[8].x};
      v[0] = zb[1]; v[8] = zb[2]; v[12] = zb[3];
#pragma unroll
      for (int i = 1; i < 8; ++i) v[i] = cm(P[2 * i], y256, y256s);
#pragma unroll
      for (int i = 1; i < 4; ++i) { v[8 + i] = cm(P[4 * i], zb[2], zb2s); v[12 + i] = cm(P[4 * i], zb[3], zb3s); }
      put(s1, v, true);
      const f32x4 dB = mm();
#pragma unroll
      for (int i = 0; i < 4; ++i) { v[2 * i] = zb[4 + i]; v[2 * i + 1] = cm(zb[4 + i], P[8], p8s); }
      v[8] = zb[8]; v[9] = (f32x2){0.f, 0.f};
      put(s1, v, false);
      const f32x4 dC = mm();
      const int h = l16;
      const int iB = h < 8 ? KD1 + h + 32 * q : KD2 + 64 * ((h - 8) >> 2) + (h & 3) + 16 * q, sB = h < 8 ? 8 : 4;
#pragma unroll
      for (int r = 0; r < 4; ++r) {
        kd[KD0 + 16 + 16 * (4 * q + r) + h] = dA[r];
        kd[iB + sB * r] = dB[r];
        if (h < 8) kd[KD3 + 32 * (h >> 1) + 2 * (4 * q + r) + (h & 1)] = dC[r];
      }
      if (q == 0) {
        if (h < 3) kd[KD0 + 16 + 256 + h + (h > 0)] = dB[0];
        if (h == 0) { kd[KD0 + 16 + 257] = dB[0]; kd[KD2 + 128] = dC[0]; kd[KD2 + 129] = dC[0]; }
        if (h == 8) { kd[KD1 + 128] = dB[0]; kd[KD1 + 129] = dB[0]; kd[KD3 + 128] = dC[0]; }
        if (h == 9) kd[KD1 + 130] = dB[0];
      }
    }
    STAMP_AT(2);
    {
      LAS u32* cnt = (LAS u32*)(sm + K12_CNT);
      WAIT_L0();
      if (lane == 0) __hip_atomic_fetch_add(cnt, 1u, __ATOMIC_RELAXED, __HIP_MEMORY_SCOPE_WORKGROUP);
      while (rfl(__hip_atomic_load(cnt, __ATOMIC_RELAXED, __HIP_MEMORY_SCOPE_WORKGROUP)) < 6u) __builtin_amdgcn_s_sleep(1);
      asm volatile("" ::: "memory");
    }
    STAMP_AT(3);
    constexpr int HB = 3 * KD_CH;
    float s[3][11];
    int ch[3], M[3];
    bool act[3];
    auto win0 = [&](int T, int i) {
      act[i] = T < 102;
      ch[i] = min(T / 34, 2);
      const int t = min(T - ch[i] * 34, 33);
      M[i] = -8 + 8 * t;
      LAS float* kd = (LAS float*)(sm + K12_KD) + ch[i] * KD_CH + KD0 + 8 * t;
      const f32x4 u = *(LAS f32x4*)kd + *(LAS f32x4*)(kd + HB), v = *(LAS f32x4*)(kd + 4) + *(LAS f32x4*)(kd + HB + 4),
                  w = *(LAS f32x4*)(kd + 8) + *(LAS f32x4*)(kd + HB + 8);
      s[i][0] = w[3]; s[i][1] = w[2]; s[i][2] = w[1]; s[i][3] = w[0]; s[i][4] = v[3]; s[i][5] = v[2]; s[i][6] = v[1]; s[i][7] = v[0];
      s[i][8] = u[3]; s[i][9] = u[2]; s[i][10] = u[1];
    };
    {
      ch[0] = wid >> 1; act[0] = true;
      const int t = (wid & 1) * 64 + lane;
      M[0] = 1032 + 8 * t;
      LAS float* kd = (LAS float*)(sm + K12_KD) + ch[0] * KD_CH + KD3 + t;
      const float b = kd[0] + kd[HB], a = kd[1] + kd[HB + 1];
#pragma unroll
      for (int i = 0; i < 7; ++i) s[0][i] = a;
      s[0][7] = (t & 1) ? a : b;
      s[0][8] = s[0][9] = s[0][10] = b;
    }
    if (wid < 3) {
      ch[1] = wid; act[1] = true;
      M[1] = 520 + 8 * lane;
      LAS float* kd = (LAS float*)(sm + K12_KD) + wid * KD_CH + KD2 + 2 * lane;
      const f32x2 u = *(LAS f32x2*)kd + *(LAS f32x2*)(kd + HB), v = *(LAS f32x2*)(kd + 2) + *(LAS f32x2*)(kd + HB + 2);
      s[1][0] = v.y; s[1][1] = s[1][2] = s[1][3] = s[1][4] = s[1][5] = v.x; s[1][6] = s[1][7] = s[1][8] = u.y; s[1][9] = s[1][10] = u.x;
    } else if (wid < 5) {
      const int T = (wid - 3) * 64 + lane;
      act[1] = T < 96;
      ch[1] = min(T >> 5, 2);
      const int t = T & 31;
      M[1] = 264 + 8 * t;
      LAS float* kd = (LAS float*)(sm + K12_KD) + ch[1] * KD_CH + KD1 + 4 * t;
      const f32x4 u = *(LAS f32x4*)kd + *(LAS f32x4*)(kd + HB), v = *(LAS f32x4*)(kd + 4) + *(LAS f32x4*)(kd + HB + 4);
      s[1][0] = v[2]; s[1][1] = v[1]; s[1][2] = s[1][3] = s[1][4] = v[0]; s[1][5] = u[3]; s[1][6] = s[1][7] = s[1][8] = u[2]; s[1][9] = u[1]; s[1][10] = u[0];
    } else {
      win0(lane, 1);
    }
    act[2] = false;
    if (wid == 0) win0(64 + lane, 2);
#pragma unroll
    for (int i = 0; i < 3; ++i) {
      if (i == 2 && wid != 0) break;
      if (act[i]) {
        u32 E[5], O[5];
#pragma unroll
        for (int k = 0; k < 5; ++k) { E[k] = cvtpk(s[i][2 * k], s[i][2 * k + 1]); O[k] = cvtpk(s[i][2 * k + 1], s[i][2 * k + 2]); }
        LAS char* dst = sm + K12_KC + ch[i] * KC_CH + 2 * (2048 - M[i]);
        *(LAS u32x4*)(dst + 3 * KC_COPY) = (u32x4){E[0], E[1], E[2], E[3]};
        *(LAS u32x4*)(dst + 2 * KC_COPY) = (u32x4){O[0], O[1], O[2], O[3]};
        *(LAS u32x4*)(dst + 1 * KC_COPY) = (u32x4){E[1], E[2], E[3], E[4]};
        *(LAS u32x4*)(dst + 0 * KC_COPY) = (u32x4){O[1], O[2], O[3], O[4]};
      }
    }
  }
  STAMP_AT(4);
  u64 g[4];
#pragma unroll
  for (int k = 0; k < 4; ++k) g[k] = __hip_atomic_load((GAS u64*)(gran + 4 * tid + k), RLX_AGENT);
  __syncthreads();
  STAMP_AT(5);

  {
    u32 spins = 0;
    for (;;) {
      bool ok = true;
#pragma unroll
      for (int k = 0; k < 4; ++k) ok &= (u32)(g[k] >> 32) == tag;
      if (__all(ok)) break;
      if (++spins > (1u << 14)) {
#pragma unroll 1
        for (int k = 0; k < 4; ++k) {
          if ((u32)(g[k] >> 32) != tag) {
            const u16* p = x + (4 * tid + k) * D_;
            float s = 0.f;
            for (int i = 0; i < D_; ++i) s += bf2f(p[i]);
            const float mean = s * (1.f / D_);
            float qs = 0.f;
            for (int i = 0; i < D_; ++i) { const float d = bf2f(p[i]) - mean; qs = __builtin_fmaf(d, d, qs); }
            const u64 v = ((u64)tag << 32) | ln_pack(mean, qs * (1.f / D_));
            if (k == 0) g[0] = v; else if (k == 1) g[1] = v; else if (k == 2) g[2] = v; else g[3] = v;
          }
        }
        break;
      }
      __builtin_amdgcn_s_sleep(1);
#pragma unroll
      for (int k = 0; k < 4; ++k) g[k] = __hip_atomic_load((GAS u64*)(gran + 4 * tid + k), RLX_AGENT);
    }
    float m[4], r[4];
#pragma unroll
    for (int k = 0; k < 4; ++k) { m[k] = bflo((u32)g[k]); r[k] = __builtin_amdgcn_rcpf(bfhi((u32)g[k])); }
#pragma unroll
    for (int c = 0; c < 3; ++c) {
      const u32x2 px = *(LAS u32x2*)(sm + K12_XS + c * 4096 + tid * 8);
      u32 o[2];
#pragma unroll
      for (int p = 0; p < 2; ++p) {
        const u32 e = px[p];
        const u32 d = cvtpk(bflo(e) - m[2 * p], bfhi(e) - m[2 * p + 1]);
        o[p] = cvtpk(bflo(d) * r[2 * p], bfhi(d) * r[2 * p + 1]);
      }
      *(LAS u32x2*)(sm + K12_US + c * 4608 + 512 + tid * 8) = (u32x2){o[0], o[1]};
    }
  }
  STAMP_AT(6);
  __syncthreads();
  STAMP_AT(7);
  if (bid == 0 && tid == 0) __hip_atomic_store((GAS u32*)epoch, tag, RLX_AGENT);
  if (wid >= 4) {
    if (wid >= 6) pack_wd();
    __syncthreads();
    return;
  }

  f32x4 accA[8], accB[8];
#pragma unroll
  for (int t = 0; t < 8; ++t) { accA[t] = (f32x4){0.f, 0.f, 0.f, 0.f}; accB[t] = accA[t]; }
  {
    const int kap = l16 & 3;
    const int kb0 = K12_KC + kap * KC_COPY + 2 * (2048 - 16 - l16 + 8 * q + kap) - 512 * 7;
    const int ub0 = K12_US + 480 + 32 * l16 + 16 * q;
    const int n1 = wid == 1 ? 1 : (wid == 2 ? 2 : 3);
    bf16x4 A0[8][2], A1[8][2];
    bf16x8 B0[8], B1[8];
    auto adr = [&](int un, LAS char*& kb, LAS char*& ub) {
      const int U = 6 * wid + min(un, 5), ch = U >> 3, r = (U & 7) * 2;
      kb = sm + kb0 + ch * KC_CH - 32 * r;
      ub = sm + ub0 + ch * 4608 - 32 * r;
    };
    auto rd = [&](int m, LAS char* kb, LAS char* ub, bf16x4 (&A)[8][2], bf16x8 (&B)[8]) {
      const int i = m / 3, k = m - 3 * i;
      if (k == 0) B[i] = *(LAS bf16x8*)(ub + 512 * i);
      else A[i][k - 1] = *(LAS bf16x4*)(kb + 512 * (7 - i) + 8 * (k - 1));
    };
    auto unit = [&](f32x4 (&acc)[8], const bf16x4 (&A)[8][2], const bf16x8 (&B)[8], bf16x4 (&An)[8][2], bf16x8 (&Bn)[8], LAS char* kb, LAS char* ub) {
      int m = 0;
#pragma unroll
      for (int d = 0; d < 8; ++d)
#pragma unroll
        for (int e = 0; e <= 2 * d; ++e) {
          const int i = e <= d ? d : 2 * d - e, j = e <= d ? e : d;
          if (i + j <= 7) {
            acc[i + j] = mfma16(cat8(A[i][0], A[i][1]), B[j], acc[i + j]);
            if (m < 24) rd(m, kb, ub, An, Bn);
            ++m;
            PIN();
          }
        }
    };
    auto pair = [&](f32x4 (&acc)[8], int it) {
      LAS char *kb, *ub;
      adr(2 * it + 1, kb, ub);
      WAIT_L0();
      unit(acc, A0, B0, A1, B1, kb, ub);
      adr(2 * it + 2, kb, ub);
      WAIT_L0();
      unit(acc, A1, B1, A0, B0, kb, ub);
    };
    {
      LAS char *kb, *ub;
      adr(0, kb, ub);
#pragma unroll
      for (int m = 0; m < 24; ++m) rd(m, kb, ub, A0, B0);
    }
#pragma unroll 1
    for (int it = 0; it < n1; ++it) pair(accA, it);
#pragma unroll 1
    for (int it = n1; it < 3; ++it) pair(accB, it);
  }
  STAMP_AT(8);
  {
    LAS char* pp = sm + K12_SCR + lane * 16;
    if (wid == 1 || wid == 2) {
#pragma unroll
      for (int t = 0; t < 8; ++t) *(LAS f32x4*)(pp + (wid - 1) * 8192 + t * 1024) = accA[t];
    }
    if (wid == 2) {
#pragma unroll
      for (int t = 0; t < 8; ++t) *(LAS f32x4*)(pp + 2 * 8192 + t * 1024) = accB[t];
    }
  }
  __syncthreads();
  STAMP_AT(9);
  if (wid != 2) {
    const int ch = wid == 3 ? 2 : wid;
    const float dk = bf2f(ch == 0 ? dsk[0] : (ch == 1 ? dsk[1] : dsk[2]));
    LAS char* pp = sm + K12_SCR + ch * 8192 + lane * 16;
    const srd_t so = mk_srd(hT, D_ * L_ * 2);
    const u32 dst = (u32)((c0 + ch) * L_ + 16 * l16 + 4 * q) * 2u;
#pragma unroll
    for (int t = 0; t < 8; ++t) {
      const f32x4 y = (wid == 1 ? accB[t] : accA[t]) + *(LAS f32x4*)(pp + t * 1024);
      const u32x2 xx = *(LAS u32x2*)(sm + K12_XS + ch * 4096 + 2 * (256 * t + 16 * l16 + 4 * q));
      const u32x2 uu = *(LAS u32x2*)(sm + K12_US + ch * 4608 + 512 + 2 * (256 * t + 16 * l16 + 4 * q));
      const float h0 = bflo(xx[0]) + __builtin_fmaf(bflo(uu[0]), dk, y[0]);
      const float h1 = bfhi(xx[0]) + __builtin_fmaf(bfhi(uu[0]), dk, y[1]);
      const float h2 = bflo(xx[1]) + __builtin_fmaf(bflo(uu[1]), dk, y[2]);
      const float h3 = bfhi(xx[1]) + __builtin_fmaf(bfhi(uu[1]), dk, y[3]);
      __builtin_amdgcn_raw_buffer_store_b64((u32x2){cvtpk(h0, h1), cvtpk(h2, h3)}, so, dst + 512 * t, 0, HT_AUX);
    }
  }
  STAMP_AT(10);
}

constexpr int G1_H = 14336, G1_STAGE = 22528, G1_NST = 6, G1_NK = 20;
constexpr int G1_ST = G1_NST * G1_STAGE, G1_CS = G1_ST + 512;
constexpr int G1_LDS = G1_CS + 224 * 4;

template <int I> struct ic_t { static constexpr int value = I; };
template <int N, int I = 0, class F> DI void static_for(F&& f) {
  if constexpr (I < N) { f(ic_t<I>{}); static_for<N, I + 1>(f); }
}
template <int N> DI void wait_vm() { asm volatile("s_waitcnt vmcnt(%0)" ::"n"(N) : "memory"); }

constexpr int g1_first(int i) { return i < 0 ? 0 : (i == 0 ? 3 : (i == 1 ? 4 : (i == 2 ? 6 : (i == 3 ? 8 : i + 5)))); }
constexpr int g1_end(int i) { const int e = g1_first(i + 1); return e < G1_NK ? e : G1_NK; }
constexpr int g1_younger(int stage, int last, int P) {
  int n = 0;
  bool seen = false;
  for (int i = -1; i <= last; ++i) {
    const int f = g1_first(i) < G1_NK ? g1_first(i) : G1_NK;
    for (int s2 = f; s2 < g1_end(i); ++s2) { if (seen) n += P; if (s2 == stage) seen = true; }
  }
  return n < 63 ? n : 63;
}

template <bool SIX>
DI void g1_loader(const u16* __restrict__ hT, const char* __restrict__ wp1, const float* __restrict__ csp, LAS char* sm,
                  const int lw, const int lane, const int fs, const int m0 STAMP_ARG2) {
  constexpr int P = SIX ? 6 : 5;
  const u32 lds0 = (u32)(size_t)sm;
  const srd_t srdW = mk_srd(wp1 + (size_t)fs * 10 * 28672, 10 * 28672);
  const srd_t srdH = mk_srd(hT + m0, (u32)(D_ * L_ - m0) * 2u);
  const u32 voW = lane * 16;
  const int hr = lane >> 4;
  const u32 voH = hr * (L_ * 2) + (((lane & 15) ^ (2 * (4 * (lw & 1) + hr))) * 16);
  const u32 pW = 1024 * lw, h0 = SIX ? lw + 2 : lw - 2;
  u32 lb = lds0, soW = pW, soH = h0 * (4 * L_ * 2);
  auto stage = [&]() {
#pragma unroll
    for (int i = 0; i < (SIX ? 4 : 3); ++i) dma16(lb + pW + 4096 * i, voW, srdW, soW + 4096 * i);
#pragma unroll
    for (int i = 0; i < 2; ++i) dma16(lb + G1_H + 1024 * (h0 + 4 * i), voH, srdH, soH + i * (16 * L_ * 2));
    soW += 14336; soH += 32 * L_ * 2;
    lb = lb == lds0 + (G1_NST - 1) * G1_STAGE ? lds0 : lb + G1_STAGE;
  };
  float cp[10];
#pragma unroll
  for (int s2 = g1_first(-1); s2 < g1_end(-1); ++s2) stage();
  wait_vm<g1_younger(0, -1, P)>();
  BAR();
  STAMP_AT(1);
  auto interval = [&](auto ic) {
    constexpr int i = decltype(ic)::value;
#pragma unroll
    for (int s2 = g1_first(i); s2 < g1_end(i); ++s2) stage();
    if (i == G1_NK - 5) {
      const int c = min(64 * lw + lane, 223);
#pragma unroll
      for (int k = 0; k < 10; ++k) cp[k] = csp[(fs * 10 + k) * 224 + c];
    }
    if (i + 1 < G1_NK) wait_vm<(g1_younger(i + 1, i, P) + (i >= G1_NK - 5 ? 10 : 0) < 63 ? g1_younger(i + 1, i, P) + (i >= G1_NK - 5 ? 10 : 0) : 63)>();
    BAR();
  };
  static_for<G1_NK>(interval);
  float cs = 0.f;
#pragma unroll
  for (int i = 0; i < 10; ++i) cs += cp[i];
  if (64 * lw + lane < 224) *(LAS float*)(sm + G1_CS + (64 * lw + lane) * 4) = cs;
  __syncthreads();
}

__global__ __launch_bounds__(512, 2) void gemm1(const u16* __restrict__ hT, const char* __restrict__ wp1, const float* __restrict__ csp,
                                                char* __restrict__ Hp, float* __restrict__ r2 STAMP_ARG) {
  STAMP_DECL();
  STAMP_AT(0);
  extern __shared__ __attribute__((aligned(16))) char smem[];
  LAS char* sm = (LAS char*)smem;
  const u32 lds0 = (u32)(size_t)sm;
  const int tid = threadIdx.x, lane = tid & 63;
  const int w = rfl(tid >> 6);
  const int bid = blockIdx.x, xcd = bid & 7, jj = bid >> 3;
  const int mt = (xcd & 1) * 8 + (jj & 7), fs = (xcd >> 1) * 4 + (jj >> 3);
  const int m0 = mt * 128;

  if (w >= 4) {
    if (w < 6) g1_loader<true>(hT, wp1, csp, sm, w - 4, lane, fs, m0 STAMP_FWD);
    else g1_loader<false>(hT, wp1, csp, sm, w - 4, lane, fs, m0 STAMP_FWD);
    return;
  }

  const int mh = w >> 1, nh = w & 1;
  const int q = lane >> 4, qq = (lane & 15) >> 2, p = lane & 3;
  u32 aW = lds0 + nh * 7168 + lane * 16;
  u32 aH[4];
#pragma unroll
  for (int t = 0; t < 4; ++t) {
    const int tt = 4 * mh + ((t + 2 * nh) & 3);
    const int X = 2 * (4 * (q & 1) + qq);
    aH[t] = lds0 + G1_H + (4 * q + qq) * 256 + (((2 * tt) ^ X) + (p >> 1)) * 16 + 8 * (p & 1);
  }
  f32x4 acc[4][7], accS[2];
#pragma unroll
  for (int t = 0; t < 4; ++t)
#pragma unroll
    for (int j = 0; j < 7; ++j) acc[t][j] = (f32x4){0.f, 0.f, 0.f, 0.f};
  accS[0] = acc[0][0]; accS[1] = acc[0][0];
  u32x4 onesv = {0x3F803F80u, 0x3F803F80u, 0x3F803F80u, 0x3F803F80u};
  asm volatile("" : "+v"(onesv));
  const bf16x8 ones = __builtin_bit_cast(bf16x8, onesv);
  bf16x8 W0[7], W1[7];
  bf16x4 H0[4][2], H1[4][2];
  auto rd = [&](int m, bf16x8 (&W)[7], bf16x4 (&H)[4][2]) {
    if (m < 8) H[m >> 1][m & 1] = trread((LAS char*)(size_t)aH[m >> 1] + (m & 1) * 4096);
    else W[m - 8] = *(LAS bf16x8*)((LAS char*)(size_t)aW + (m - 8) * 1024);
  };
  const int sr = lane & 31;
  u32 sa = lds0 + G1_H + sr * 256 + ((fs ^ (2 * (sr & 7))) * 16) + w * 4, sv, svn, slo, shi;
  float a1 = 0.f, b1 = 0.f, a2 = 0.f, b2 = 0.f;
  auto step = [&](const bf16x8 (&W)[7], const bf16x4 (&H)[4][2], bf16x8 (&Wn)[7], bf16x4 (&Hn)[4][2], u32 adv, const u32& s_cur, u32& s_nxt) {
#pragma unroll
    for (int m = 0; m < 30; ++m) {
      const int j = m >> 2, t = m & 3;
      if (m < 28) acc[t][j] = mfma16(W[j], cat8(H[t][0], H[t][1]), acc[t][j]);
      else accS[m - 28] = mfma16(ones, cat8(H[m - 28][0], H[m - 28][1]), accS[m - 28]);
      if (m < 15) rd(m, Wn, Hn);
      if (m == 15) s_nxt = *(LAS u32*)((LAS char*)(size_t)sa);
      if (m == 16) vadd_s(aW, adv);
      if (m >= 17 && m < 21) vadd_s(aH[m - 17], adv);
      if (m == 21) vadd_s(sa, adv);
      if (m == 22) vshl16(slo, s_cur);
      if (m == 23) vhi16(shi, s_cur);
      if (m == 24) vaddf(a1, slo);
      if (m == 25) vaddf(b1, shi);
      if (m == 26) vsq(a2, slo);
      if (m == 27) vsq(b2, shi);
      PIN();
    }
  };
  auto advance = [&](int ks) {
    return ((ks + 2) % G1_NST == 0) ? (u32)(-(G1_NST - 1) * G1_STAGE) : (u32)G1_STAGE;
  };
  BAR();
  STAMP_AT(2);
  WAIT_L0();
#pragma unroll
  for (int m = 0; m < 15; ++m) rd(m, W0, H0);
  sv = *(LAS u32*)((LAS char*)(size_t)sa);
  aW += G1_STAGE; sa += G1_STAGE;
#pragma unroll
  for (int t = 0; t < 4; ++t) aH[t] += G1_STAGE;
#pragma unroll 1
  for (int ks = 0; ks < G1_NK; ks += 2) {
    WAIT_L0();
    BAR();
    step(W0, H0, W1, H1, advance(ks), sv, svn);
    WAIT_L0();
    BAR();
    step(W1, H1, W0, H0, advance(ks + 1), svn, sv);
  }
  STAMP_AT(6);
  LAS float* st = (LAS float*)(sm + G1_ST);
  if (lane < 16) {
#pragma unroll
    for (int t = 0; t < 2; ++t) st[(4 * mh + ((t + 2 * nh) & 3)) * 16 + lane] = rbf(accS[t][0] * (1.f / D_));
  }
  {
    a1 = dpp_add<0x128>(a1); b1 = dpp_add<0x128>(b1); a2 = dpp_add<0x128>(a2); b2 = dpp_add<0x128>(b2);
    a1 = dpp_add<0x124>(a1); b1 = dpp_add<0x124>(b1); a2 = dpp_add<0x124>(a2); b2 = dpp_add<0x124>(b2);
    a1 = dpp_add<0x122>(a1); b1 = dpp_add<0x122>(b1); a2 = dpp_add<0x122>(a2); b2 = dpp_add<0x122>(b2);
    a1 = dpp_add<0x121>(a1); b1 = dpp_add<0x121>(b1); a2 = dpp_add<0x121>(a2); b2 = dpp_add<0x121>(b2);
    float s1 = (lane & 1) ? b1 : a1, s2 = (lane & 1) ? b2 : a2;
    { auto r = __builtin_amdgcn_permlane16_swap(__float_as_uint(s1), __float_as_uint(s1), false, false); s1 = __uint_as_float(r[0]) + __uint_as_float(r[1]); }
    { auto r = __builtin_amdgcn_permlane16_swap(__float_as_uint(s2), __float_as_uint(s2), false, false); s2 = __uint_as_float(r[0]) + __uint_as_float(r[1]); }
    const float mn = s1 * (1.f / D_);
    const float var = fmaxf(s2 * (1.f / D_) - mn * mn, 0.f);
    const float rs = 1.f / bfhi(ln_pack(mn, var));
    if (lane < 2) r2[m0 + 8 * fs + 2 * w + lane] = rs * rs;
  }
  __syncthreads();
  float mean[4];
#pragma unroll
  for (int t = 0; t < 4; ++t) mean[t] = st[(4 * mh + ((t + 2 * nh) & 3)) * 16 + (lane & 15)];
  const int g0 = ((27 * fs) >> 1) + 7 * nh;
  const srd_t so = mk_srd(Hp, 32 * 54 * 4096);
  f32x4 cv[7];
#pragma unroll
  for (int j = 0; j < 7; ++j) cv[j] = *(LAS f32x4*)(sm + G1_CS + ((7 * nh + j) * 16 + 4 * q) * 4);
#pragma unroll
  for (int pr = 0; pr < 2; ++pr) {
    const int tt = 4 * mh + ((2 * pr + (q & 1) + 2 * nh) & 3);
    const u32 base = (u32)((2 * mt + (tt >> 2)) * 54 * 4096 + (tt & 3) * 1024 + (lane & 15) * 16);
    u32 pk[7][2];
#pragma unroll
    for (int j = 0; j < 7; ++j) {
      const f32x4 a = __builtin_elementwise_fma(cv[j], (f32x4)(-mean[2 * pr]), acc[2 * pr][j]);
      const f32x4 b = __builtin_elementwise_fma(cv[j], (f32x4)(-mean[2 * pr + 1]), acc[2 * pr + 1][j]);
      f32x4 gv, uv;
#pragma unroll
      for (int r = 0; r < 4; ++r) {
        auto s = __builtin_amdgcn_permlane16_swap(__float_as_uint(a[r]), __float_as_uint(b[r]), false, false);
        gv[r] = __uint_as_float(s[0]); uv[r] = __uint_as_float(s[1]);
      }
      const f32x4 pv = gv * uv;
      pk[j][0] = cvtpk(pv[0], pv[1]); pk[j][1] = cvtpk(pv[2], pv[3]);
      if (j & 1) {
        auto s0 = __builtin_amdgcn_permlane32_swap(pk[j - 1][0], pk[j][0], false, false);
        auto s1x = __builtin_amdgcn_permlane32_swap(pk[j - 1][1], pk[j][1], false, false);
        const int gi = g0 + j - 1 + (q >> 1);
        __builtin_amdgcn_raw_buffer_store_b128((u32x4){s0[0], s1x[0], s0[1], s1x[1]}, so, base + (gi >> 2) * 4096 + (gi & 3) * 256, 0, H_AUX);
      }
    }
    const int gi = g0 + 6;
    __builtin_amdgcn_raw_buffer_store_b64((u32x2){pk[6][0], pk[6][1]}, so, base + (gi >> 2) * 4096 + (gi & 3) * 256 + 8 * (q >> 1), 0, H_AUX);
  }
  STAMP_AT(7);
}

constexpr int G2_H = 10240, G2_STAGE = 18432, G2_NST = 8, G2_NK = 27, G2_RES = G2_NST * G2_STAGE;
constexpr int G2_LDS = G2_RES + 10240;

constexpr int g2_first(int j) { return j < 0 ? 0 : (j == 0 ? 4 : (j == 1 ? 5 : (j == 2 ? 9 : 2 * j + 5))); }
constexpr int g2_end(int j) { const int e = g2_first(j + 1); return e < G2_NK ? e : G2_NK; }
constexpr int G2_RESJ = 6;
constexpr int g2_younger(int tile, int last, int P) {
  int n = 0;
  bool seen = false;
  for (int j = -1; j <= last; ++j) {
    if (seen && j == G2_RESJ) n += 3;
    const int f = g2_first(j) < G2_NK ? g2_first(j) : G2_NK;
    for (int t = f; t < g2_end(j); ++t) { if (seen) n += P; if (t == tile) seen = true; }
  }
  return n < 63 ? n : 63;
}

template <bool ISW>
DI void g2_loader(const char* __restrict__ Hp, const char* __restrict__ wp2, const u16* __restrict__ hT, LAS char* sm, const int lw, const int lane,
                  const int mt, const int nt STAMP_ARG2) {
  constexpr int P = ISW ? 5 : 4;
  const u32 lds0 = (u32)(size_t)sm;
  const srd_t srd = ISW ? mk_srd(wp2 + (size_t)nt * 27 * 10240, 27 * 10240) : mk_srd(Hp + (size_t)mt * 54 * 4096, 54 * 4096);
  const u32 vo = lane * 16;
  const u32 l0 = lds0 + (ISW ? 5120 * lw : G2_H + 4096 * (lw - 2));
  u32 lb = l0, so = ISW ? 5120 * lw : 4096 * (lw - 2);
  auto tile = [&]() {
#pragma unroll
    for (int i = 0; i < P; ++i) dma16(lb + 1024 * i, vo, srd, so + 1024 * i);
    so += ISW ? 10240 : 8192;
    lb = lb == l0 + (G2_NST - 1) * G2_STAGE ? l0 : lb + G2_STAGE;
  };
#pragma unroll
  for (int t = g2_first(-1); t < g2_end(-1); ++t) tile();
  wait_vm<g2_younger(0, -1, P)>();
  BAR();
  STAMP_AT(1);
  auto interval = [&](auto jc) {
    constexpr int j = decltype(jc)::value;
    if (j == G2_RESJ) {
      const srd_t srdR = mk_srd(hT + (size_t)nt * 80 * L_ + mt * 64, (u32)((D_ - nt * 80) * L_ - mt * 64) * 2u);
#pragma unroll
      for (int i = 0; i < 3; ++i) {
        const int pc = min(lw + 4 * i, 9);
        dma16(lds0 + G2_RES + 1024 * pc, (u32)((8 * pc + (lane >> 3)) * (L_ * 2) + (lane & 7) * 16), srdR, 0u);
      }
    }
#pragma unroll
    for (int t = g2_first(j); t < g2_end(j); ++t) tile();
    constexpr int need = 2 * j + 2 < G2_NK ? 2 * j + 2 : G2_NK - 1;
    if (j >= 12) wait_vm<0>(); else wait_vm<g2_younger(need, j, P)>();
    BAR();
  };
  static_for<14>(interval);
  __syncthreads();
}

__global__ __launch_bounds__(512, 2) void gemm2(const char* __restrict__ Hp, const char* __restrict__ wp2, const u16* __restrict__ hT,
                                                const float* __restrict__ r2, u16* __restrict__ out STAMP_ARG) {
  STAMP_DECL();
  STAMP_AT(0);
  extern __shared__ __attribute__((aligned(16))) char smem[];
  LAS char* sm = (LAS char*)smem;
  const u32 lds0 = (u32)(size_t)sm;
  const int tid = threadIdx.x, lane = tid & 63;
  const int w = rfl(tid >> 6);
  const int bid = blockIdx.x, xcd = bid & 7, jj = bid >> 3;
  const int fg = xcd >> 1;
  const int mt = (xcd & 1) * 16 + (fg >> 1) * 8 + (jj & 7), nt = (fg & 1) * 4 + (jj >> 3);
  const int m0 = mt * 64, n0 = nt * 80;

  if (w >= 4) {
    if (w < 6) g2_loader<true>(Hp, wp2, hT, sm, w - 4, lane, mt, nt STAMP_FWD);
    else g2_loader<false>(Hp, wp2, hT, sm, w - 4, lane, mt, nt STAMP_FWD);
    return;
  }

  const int th = w >> 1, kh = w & 1;
  u32 aW = lds0 + kh * 5120 + lane * 16, aH = lds0 + G2_H + kh * 4096 + th * 2048 + lane * 16;
  f32x4 acc[2][5];
#pragma unroll
  for (int t = 0; t < 2; ++t)
#pragma unroll
    for (int d = 0; d < 5; ++d) acc[t][d] = (f32x4){0.f, 0.f, 0.f, 0.f};
  const float rsc = r2[m0 + 16 * (2 * th + kh) + (lane & 15)];
  bf16x8 W0[5], W1[5], H0[2], H1[2];
  auto rd = [&](int m, bf16x8 (&W)[5], bf16x8 (&H)[2]) {
    if (m < 2) H[m] = *(LAS bf16x8*)((LAS char*)(size_t)aH + m * 1024);
    else W[m - 2] = *(LAS bf16x8*)((LAS char*)(size_t)aW + (m - 2) * 1024);
  };
  int rb = 0;
  auto next = [&]() {
    const u32 adv = rb == G2_NST - 1 ? (u32)(-(G2_NST - 1) * G2_STAGE) : (u32)G2_STAGE;
    rb = rb == G2_NST - 1 ? 0 : rb + 1;
    return adv;
  };
  auto step = [&](const bf16x8 (&W)[5], const bf16x8 (&H)[2], bf16x8 (&Wn)[5], bf16x8 (&Hn)[2]) {
    const u32 adv = next();
#pragma unroll
    for (int m = 0; m < 10; ++m) {
      acc[m & 1][m >> 1] = mfma16(W[m >> 1], H[m & 1], acc[m & 1][m >> 1]);
      if (m < 7) rd(m, Wn, Hn);
      if (m == 7) vadd_s(aW, adv);
      if (m == 8) vadd_s(aH, adv);
      PIN();
    }
  };
  BAR();
  STAMP_AT(2);
  WAIT_L0();
  {
#pragma unroll
    for (int m = 0; m < 7; ++m) rd(m, W0, H0);
    const u32 adv = next(); aW += adv; aH += adv;
  }
#pragma unroll 1
  for (int j = 0; j < 13; ++j) {
    WAIT_L0();
    BAR();
    step(W0, H0, W1, H1);
    WAIT_L0();
    step(W1, H1, W0, H0);
  }
  WAIT_L0();
  BAR();
#pragma unroll
  for (int m = 0; m < 10; ++m) acc[m & 1][m >> 1] = mfma16(W0[m >> 1], H0[m & 1], acc[m & 1][m >> 1]);
  STAMP_AT(6);
  {
    LAS char* ex = sm + w * 5120 + lane * 16;
#pragma unroll
    for (int d = 0; d < 5; ++d) *(LAS f32x4*)(ex + d * 1024) = kh ? acc[0][d] : acc[1][d];
  }
  __syncthreads();
  const int q = lane >> 4, qq = (lane & 15) >> 2, p = lane & 3;
  const int tt = 2 * th + kh;
  u16* orow = out + (size_t)(m0 + 16 * tt + (lane & 15)) * D_ + n0 + 4 * q;
#pragma unroll
  for (int d = 0; d < 5; ++d) {
    const f32x4 o = ((kh ? acc[1][d] : acc[0][d]) + *(LAS f32x4*)(sm + (w ^ 1) * 5120 + lane * 16 + d * 1024)) * rsc;
    const u32x2 hr = __builtin_bit_cast(u32x2, trread(sm + G2_RES + (16 * d + 4 * q + qq) * 128 + 32 * tt + 8 * p));
    const float o0 = bflo(hr[0]) + o[0], o1 = bfhi(hr[0]) + o[1], o2 = bflo(hr[1]) + o[2], o3 = bfhi(hr[1]) + o[3];
    *(u32x2*)(orow + 16 * d) = (u32x2){cvtpk(o0, o1), cvtpk(o2, o3)};
  }
  STAMP_AT(7);
}

constexpr size_t WS_STAMP = 32u << 20;
constexpr size_t WS_EPOCH = 0, WS_GRAN = 4096, WS_R2 = WS_GRAN + 16384, WS_CS = WS_R2 + 8192, WS_HT = 262144, WS_H = WS_HT + (size_t)D_ * L_ * 2,
                 WS_WP1 = WS_H + (size_t)32 * 54 * 4096, WS_WP2 = WS_WP1 + (size_t)16 * 10 * 28672;

extern "C" void kernel_launch(void* const* d_in, const int* in_sizes, int n_in, void* d_out, int out_size, void* d_ws, size_t ws_size,
                              hipStream_t stream) {
  char* ws = (char*)d_ws;
  const u16* x = (const u16*)d_in[0];
  u16* hT = (u16*)(ws + WS_HT);
  float* csp = (float*)(ws + WS_CS);
  (void)hipFuncSetAttribute((const void*)k12, hipFuncAttributeMaxDynamicSharedMemorySize, K12_LDS);
  (void)hipFuncSetAttribute((const void*)gemm1, hipFuncAttributeMaxDynamicSharedMemorySize, G1_LDS);
  (void)hipFuncSetAttribute((const void*)gemm2, hipFuncAttributeMaxDynamicSharedMemorySize, G2_LDS);
  k12<<<256, 512, K12_LDS, stream>>>(x, (const u16*)d_in[1], (const u16*)d_in[2], (const u16*)d_in[3], (const u16*)d_in[4],
                                     (const u16*)d_in[5], (const u16*)d_in[6], (const u16*)d_in[7], (const u16*)d_in[8],
                                     (const u16*)d_in[10], (const u16*)d_in[11], (const u16*)d_in[12], hT, ws + WS_WP1, ws + WS_WP2, csp,
                                     (u64*)(ws + WS_GRAN), (u32*)(ws + WS_EPOCH) STAMP_PASS(0));
  gemm1<<<256, 512, G1_LDS, stream>>>(hT, ws + WS_WP1, csp, ws + WS_H, (float*)(ws + WS_R2) STAMP_PASS(1));
  gemm2<<<256, 512, G2_LDS, stream>>>(ws + WS_H, ws + WS_WP2, hT, (const float*)(ws + WS_R2), (u16*)d_out STAMP_PASS(2));
}
```

```cpp
#include <hip/hip_runtime.h>
#include <stdint.h>

typedef unsigned short u16;
typedef unsigned int u32;
typedef unsigned long long u64;
typedef float f32x2 __attribute__((ext_vector_type(2)));
typedef float f32x4 __attribute__((ext_vector_type(4)));
typedef __bf16 bf16x2 __attribute__((ext_vector_type(2)));
typedef __bf16 bf16x4 __attribute__((ext_vector_type(4)));
typedef __bf16 bf16x8 __attribute__((ext_vector_type(8)));
typedef u32 u32x2 __attribute__((ext_vector_type(2)));
typedef u32 u32x4 __attribute__((ext_vector_type(4)));
#define LAS __attribute__((address_space(3)))
#define GAS __attribute__((address_space(1)))
#define DI __device__ __forceinline__
#define RLX_AGENT __ATOMIC_RELAXED, __HIP_MEMORY_SCOPE_AGENT

#define STAMP_ARG
#define STAMP_PASS(k)
#define STAMP_DECL()
#define STAMP_FWD
#define STAMP_ARG2
#define STAMP_AT(i)
#define STAMP_FLUSH()
constexpr int L_ = 2048, D_ = 640, NS_ = 128, F_ = 1728;

DI float bflo(u32 v) { return __uint_as_float(v << 16); }
DI float bfhi(u32 v) { return __uint_as_float(v & 0xffff0000u); }
DI float bf2f(u16 v) { return __uint_as_float((u32)v << 16); }
DI u32 cvtpk(float lo, float hi) {
  f32x2 v = {lo, hi};
  bf16x2 b = __builtin_convertvector(v, bf16x2);
  return __builtin_bit_cast(u32, b);
}
DI float rbf(float x) { return bflo(cvtpk(x, x)); }
DI u32 rfl(u32 x) { return __builtin_amdgcn_readfirstlane(x); }

template <int CTRL>
DI float dpp_add(float v) {
  int t = __builtin_amdgcn_update_dpp(0, __float_as_int(v), CTRL, 0xf, 0xf, true);
  return v + __int_as_float(t);
}
#define CM_MUL(r, a, b) "v_pk_mul_f32 " r ", " a ", " b " op_sel:[0,0] op_sel_hi:[0,1]\n\t"
#define CM_FMA(r, a, b) "v_pk_fma_f32 " r ", " a ", " b ", " r " op_sel:[1,1,0] op_sel_hi:[1,0,1] neg_lo:[0,1,0]\n\t"
DI void cmul2(f32x2& r0, f32x2 a0, f32x2 b0, f32x2& r1, f32x2 a1, f32x2 b1) {
  asm(CM_MUL("%0", "%2", "%3") CM_MUL("%1", "%4", "%5") CM_FMA("%0", "%2", "%3") CM_FMA("%1", "%4", "%5")
      : "=&v"(r0), "=&v"(r1) : "v"(a0), "v"(b0), "v"(a1), "v"(b1));
}
DI void cmul3(f32x2& r0, f32x2 a0, f32x2 b0, f32x2& r1, f32x2 a1, f32x2 b1, f32x2& r2, f32x2 a2, f32x2 b2) {
  asm(CM_MUL("%0", "%3", "%4") CM_MUL("%1", "%5", "%6") CM_MUL("%2", "%7", "%8") CM_FMA("%0", "%3", "%4") CM_FMA("%1", "%5", "%6") CM_FMA("%2", "%7", "%8")
      : "=&v"(r0), "=&v"(r1), "=&v"(r2) : "v"(a0), "v"(b0), "v"(a1), "v"(b1), "v"(a2), "v"(b2));
}
DI void cmul4(f32x2& r0, f32x2 a0, f32x2& r1, f32x2 a1, f32x2& r2, f32x2 a2, f32x2& r3, f32x2 a3, f32x2 b) {
  asm(CM_MUL("%0", "%4", "%8") CM_MUL("%1", "%5", "%8") CM_MUL("%2", "%6", "%8") CM_MUL("%3", "%7", "%8")
      CM_FMA("%0", "%4", "%8") CM_FMA("%1", "%5", "%8") CM_FMA("%2", "%6", "%8") CM_FMA("%3", "%7", "%8")
      : "=&v"(r0), "=&v"(r1), "=&v"(r2), "=&v"(r3) : "v"(a0), "v"(a1), "v"(a2), "v"(a3), "v"(b));
}
DI f32x2 zpow(float ar, float ai, float p) {
  const float er = __builtin_amdgcn_exp2f(ar * p * 1.44269504088896341f);
  const float t = ai * p;
  const float CH = 0.15915494309189535f, CL = 6.4206383e-09f;
  const float hi = t * CH;
  float lo = __builtin_fmaf(t, CH, -hi);
  lo = __builtin_fmaf(t, CL, lo);
  const float fr = __builtin_amdgcn_fractf(hi) + lo;
  f32x2 r = {er * __builtin_amdgcn_cosf(fr), er * __builtin_amdgcn_sinf(fr)};
  return r;
}

DI bf16x8 cat8(bf16x4 a, bf16x4 b) { return __builtin_shufflevector(a, b, 0, 1, 2, 3, 4, 5, 6, 7); }
DI bf16x4 trread(LAS char* p) { return __builtin_amdgcn_ds_read_tr16_b64_v4bf16((LAS bf16x4*)p); }
DI f32x4 mfma16(bf16x8 a, bf16x8 b, f32x4 c) { return __builtin_amdgcn_mfma_f32_16x16x32_bf16(a, b, c, 0, 0, 0); }

DI u32 ln_pack(float mean, float var) {
  const float vb = rbf(var);
  const float t = rbf(vb + 1e-5f);
  const float s = __builtin_sqrtf(t);
  return cvtpk(mean, s);
}

typedef __amdgpu_buffer_rsrc_t srd_t;
DI srd_t mk_srd(const void* p, u32 bytes) { return __builtin_amdgcn_make_buffer_rsrc((void*)p, (short)0, (int)bytes, 0x00020000); }
DI void dma16(u32 lds_addr, u32 voff, srd_t srd, u32 soff) {
  asm volatile("s_mov_b32 m0, %0\n\ts_nop 0\n\tbuffer_load_dwordx4 %1, %2, %3 offen lds" ::"s"(lds_addr), "v"(voff), "s"(srd), "s"(soff) : "memory");
}
DI void touch(u32& sink, u32 voff, srd_t srd, u32 soff) {
  asm volatile("buffer_load_dword %0, %1, %2, %3 offen" : "+v"(sink) : "v"(voff), "s"(srd), "s"(soff) : "memory");
}
DI void touch_done(u32& sink) { asm volatile("s_waitcnt vmcnt(0)" : "+v"(sink)::"memory"); }
#define WAIT_V(n) asm volatile("s_waitcnt vmcnt(" #n ")" ::: "memory")
#define WAIT_L0() __builtin_amdgcn_s_waitcnt(0xC07F)
#define BAR() __builtin_amdgcn_s_barrier()
#define PIN() __builtin_amdgcn_sched_barrier(0)
DI void vadd_s(u32& a, u32 sadv) { asm volatile("v_add_u32 %0, %1, %0" : "+v"(a) : "s"(sadv)); }
DI void vshl16(u32& d, u32 x) { asm volatile("v_lshlrev_b32 %0, 16, %1" : "=v"(d) : "v"(x)); }
DI void vhi16(u32& d, u32 x) { asm volatile("v_and_b32 %0, 0xffff0000, %1" : "=v"(d) : "v"(x)); }
DI void vaddf(float& a, u32 x) { asm volatile("v_add_f32 %0, %1, %0" : "+v"(a) : "v"(x)); }
DI void vsq(float& a, u32 x) { asm volatile("v_fmac_f32 %0, %1, %1" : "+v"(a) : "v"(x)); }
#define PACK_AUX 16
#define HT_AUX 16
#define H_AUX 16

constexpr int KC_COPY = 4160;
constexpr int KC_CH = 4 * KC_COPY;
constexpr int K12_KC = 0;
constexpr int K12_US = 49920;
constexpr int K12_XS = K12_US + 3 * 4608;
constexpr int KD_CH = 684, KD0 = 0, KD1 = 288, KD2 = 420, KD3 = 552;
constexpr int K12_KD = K12_XS + 3 * 4096;
constexpr int K12_SCR = K12_KD + 6 * KD_CH * 4;
constexpr int K12_WD = K12_SCR + 6 * 8192;
constexpr int K12_CNT = K12_WD + 10240;
constexpr int K12_LDS = K12_CNT + 32;
constexpr int NCONV = 214;
constexpr int PK_U = 14352, PK_SLAB = 28928;

__global__ __launch_bounds__(512, 2) void k12(const u16* __restrict__ x, const u16* __restrict__ Are, const u16* __restrict__ Aim,
                                              const u16* __restrict__ Bre, const u16* __restrict__ Bim, const u16* __restrict__ Cre,
                                              const u16* __restrict__ Cim, const u16* __restrict__ logdt, const u16* __restrict__ Dsk,
                                              const u16* __restrict__ wg, const u16* __restrict__ wu, const u16* __restrict__ wd,
                                              u16* __restrict__ hT, char* __restrict__ wp1, char* __restrict__ wp2,
                                              float* __restrict__ csp, u64* gran, u32* epoch STAMP_ARG) {
  STAMP_DECL();
  STAMP_AT(0);
  extern __shared__ __attribute__((aligned(16))) char smem[];
  LAS char* sm = (LAS char*)smem;
  const u32 lds0 = (u32)(size_t)sm;
  const int tid = threadIdx.x, lane = tid & 63;
  const int wid = rfl(tid >> 6);
  const int bid = blockIdx.x, xcd = bid & 7, slot = bid >> 3;
  const int wq = xcd * 27 + slot;
  const bool is_conv = slot < 27 && wq < NCONV;
  const int c0 = min(3 * wq, D_ - 3);
  const int q = lane >> 4, l16 = lane & 15;
  const int tok = bid * 8 + 4 * (wid & 1) + q;
  u32x2 xr[10];
  if (wid == 2 || wid == 3) {
    const u32x2* p = (const u32x2*)(x + tok * D_);
#pragma unroll
    for (int i = 0; i < 10; ++i) xr[i] = p[l16 + 16 * i];
  }
  u32 pa[6], ldt = 0;
  if (is_conv && wid < 6) {
    const int o = (c0 + (wid >> 1)) * NS_ + (wid & 1) * 64 + lane;
    pa[0] = Are[o]; pa[1] = Aim[o]; pa[2] = Bre[o]; pa[3] = Bim[o]; pa[4] = Cre[o]; pa[5] = Cim[o];
    ldt = logdt[c0 + (wid >> 1)];
  }
  const u32 tag = *(const GAS u32*)epoch + 1u;
  const int e0 = min((c0 & ~1), D_ - 4), sh = c0 - e0;
  BAR();
  u32x2 xv[4][4];
  if (is_conv && wid >= 6) {
#pragma unroll
    for (int g4 = 0; g4 < 4; ++g4)
#pragma unroll
      for (int k = 0; k < 4; ++k) xv[g4][k] = *(const u32x2*)(x + (4 * (tid - 384 + 128 * g4) + k) * D_ + e0);
  }
  if (wid == 2 || wid == 3) {
#pragma unroll
    for (int i = 0; i < 10; ++i) asm volatile("" : "+v"(xr[i]));
  }

  if (wid == 2 || wid == 3) {
    const float piv = bflo(__builtin_amdgcn_update_dpp(0u, xr[0][0], 0x150, 0xf, 0xf, true));
    float s = 0.f, qs = 0.f;
#pragma unroll
    for (int i = 0; i < 10; ++i) {
      const float a = bflo(xr[i][0]) - piv, b = bfhi(xr[i][0]) - piv, c = bflo(xr[i][1]) - piv, d = bfhi(xr[i][1]) - piv;
      s += (a + b) + (c + d);
      qs = __builtin_fmaf(a, a, __builtin_fmaf(b, b, __builtin_fmaf(c, c, __builtin_fmaf(d, d, qs))));
    }
    s = dpp_add<0x128>(s); qs = dpp_add<0x128>(qs);
    s = dpp_add<0x124>(s); qs = dpp_add<0x124>(qs);
    s = dpp_add<0x122>(s); qs = dpp_add<0x122>(qs);
    s = dpp_add<0x121>(s); qs = dpp_add<0x121>(qs);
    const float md = s * (1.f / D_);
    const float var = fmaxf(qs * (1.f / D_) - md * md, 0.f);
    const u32 st = ln_pack(piv + md, var);
    if (l16 == 0) __hip_atomic_store((GAS u64*)(gran + tok), ((u64)tag << 32) | st, RLX_AGENT);
  }
  STAMP_AT(1);

  if (slot >= 27) {
    const int idx = (xcd & 1) * 5 + (slot - 27), fg = xcd >> 1;
    const srd_t sg = mk_srd(wg, D_ * F_ * 2), su = mk_srd(wu, D_ * F_ * 2);
    {
      const int s = idx * 4 + (wid >> 1), fs = 4 * fg + s / 10, kt = s % 10, f0 = 8 * ((27 * fs) >> 1);
      const u32 so = (u32)(kt * 64 * F_ + f0) * 2u, lb = lds0 + (wid >> 1) * PK_SLAB + (wid & 1) * PK_U;
#pragma unroll
      for (int i = 0; i < 14; ++i) {
        const int S = 64 * i + lane, rho = S / 14, sgm = S - 14 * rho;
        dma16(lb + 1024 * i, (u32)(rho * (F_ * 2) + sgm * 16), (wid & 1) ? su : sg, so);
      }
    }
    WAIT_V(0);
    BAR();
    const srd_t sp = mk_srd(wp1, 16 * 10 * 28672);
    const int p = lane & 3, qq = l16 >> 2;
    const int rd = (p & 1) * PK_U + (4 * q + qq) * 224 + 8 * (p >> 1);
    const bf16x2 ones = {(__bf16)1.0f, (__bf16)1.0f};
#pragma unroll 1
    for (int it = 0; it < 7; ++it) {
      const int job = it * 8 + wid;
      const int sl = job / 14, ct = job - 14 * sl;
      const int s = idx * 4 + sl, fs = 4 * fg + s / 10, kt = s % 10;
      LAS char* b = sm + sl * PK_SLAB + rd + ct * 16;
      float cs = 0.f;
#pragma unroll
      for (int ks = 0; ks < 2; ++ks) {
        const bf16x8 wf = cat8(trread(b + ks * 7168), trread(b + ks * 7168 + 3584));
        const u32x4 wv = __builtin_bit_cast(u32x4, wf);
#pragma unroll
        for (int i = 0; i < 4; ++i) { const u32 e = wv[i]; cs = __builtin_amdgcn_fdot2_f32_bf16(__builtin_bit_cast(bf16x2, e), ones, cs, false); }
        __builtin_amdgcn_raw_buffer_store_b128(wv, sp, (u32)((((fs * 10 + kt) * 2 + ks) * 14 + ct) * 1024 + lane * 16), 0, PACK_AUX);
      }
      { auto r = __builtin_amdgcn_permlane16_swap(__float_as_uint(cs), __float_as_uint(cs), false, false); cs = __uint_as_float(r[0]) + __uint_as_float(r[1]); }
      { auto r = __builtin_amdgcn_permlane32_swap(__float_as_uint(cs), __float_as_uint(cs), false, false); cs = __uint_as_float(r[0]) + __uint_as_float(r[1]); }
      if (lane < 16) csp[(fs * 10 + kt) * 224 + ct * 16 + lane] = cs;
    }
    return;
  }

  auto pack_wd = [&]() {
    const int ks = wid - 6;
    const srd_t sd = mk_srd(wd, F_ * D_ * 2);
    const u32 so = (u32)((slot * 64 + ks * 32) * D_ + xcd * 80) * 2u, lb = lds0 + K12_WD + ks * 5120;
#pragma unroll
    for (int i = 0; i < 5; ++i) {
      const int S = 64 * i + lane, rho = S / 10, sgm = S - 10 * rho;
      dma16(lb + 1024 * i, (u32)(rho * (D_ * 2) + sgm * 16), sd, so);
    }
    WAIT_V(0);
    const srd_t sp = mk_srd(wp2, 8 * 27 * 10240);
    const int p = lane & 3, qq = l16 >> 2;
    LAS char* b = sm + K12_WD + ks * 5120 + (8 * q + qq) * 160 + 8 * p;
#pragma unroll
    for (int dt = 0; dt < 5; ++dt) {
      const bf16x8 wf = cat8(trread(b + dt * 32), trread(b + 4 * 160 + dt * 32));
      __builtin_amdgcn_raw_buffer_store_b128(__builtin_bit_cast(u32x4, wf), sp, (u32)((((xcd * 27 + slot) * 2 + ks) * 5 + dt) * 1024 + lane * 16), 0, PACK_AUX);
    }
  };
  if (!is_conv) {
    if (wid >= 6) pack_wd();
    return;
  }

  if (wid >= 6) {
    const int t2 = tid - 384;
#pragma unroll
    for (int g4 = 0; g4 < 4; ++g4) {
      u32 lo[4], hi[4];
#pragma unroll
      for (int k = 0; k < 4; ++k) {
        lo[k] = sh ? __builtin_amdgcn_alignbit(xv[g4][k][1], xv[g4][k][0], 16) : xv[g4][k][0];
        hi[k] = sh ? (xv[g4][k][1] >> 16) : xv[g4][k][1];
      }
      LAS char* dst = sm + K12_XS + (t2 + 128 * g4) * 8;
      *(LAS u32x2*)(dst) = (u32x2){__builtin_amdgcn_perm(lo[1], lo[0], 0x05040100u), __builtin_amdgcn_perm(lo[3], lo[2], 0x05040100u)};
      *(LAS u32x2*)(dst + 4096) = (u32x2){__builtin_amdgcn_perm(lo[1], lo[0], 0x07060302u), __builtin_amdgcn_perm(lo[3], lo[2], 0x07060302u)};
      *(LAS u32x2*)(dst + 8192) = (u32x2){__builtin_amdgcn_perm(hi[1], hi[0], 0x05040100u), __builtin_amdgcn_perm(hi[3], hi[2], 0x05040100u)};
    }
  } else {
    {
      asm volatile("" : "+v"(ldt), "+v"(pa[0]), "+v"(pa[1]), "+v"(pa[2]), "+v"(pa[3]), "+v"(pa[4]), "+v"(pa[5]));
      STAMP_AT(14);
      const float dt = rbf(expf(bflo(ldt)));
      const float ar = dt * bflo(pa[0]), ai = dt * bflo(pa[1]);
      const float br = dt * bflo(pa[2]), bi = dt * bflo(pa[3]);
      const float cr = bflo(pa[4]), ci = bflo(pa[5]);
      const f32x2 c = {cr * br - ci * bi, cr * bi + ci * br};
      const f32x2 z16 = zpow(ar, ai, 16.f), y1 = zpow(ar, -ai, 1.f), y256 = zpow(ar, -ai, 256.f);
      STAMP_AT(11);
      LAS char* s0 = sm + K12_SCR + wid * 8192;
      LAS char* s1 = s0 + 4096;
      const int sw = ((lane >> 2) & 1) * 16;
      auto put = [&](LAS char* base, const f32x2 (&v)[16], bool both) {
        u32x4 a, b, d, e;
#pragma unroll
        for (int i = 0; i < 4; ++i) {
          a[i] = cvtpk(v[2 * i].x, v[2 * i + 1].x); d[i] = cvtpk(v[2 * i].y, v[2 * i + 1].y);
          if (both || i == 0) { b[i] = cvtpk(v[8 + 2 * i].x, v[9 + 2 * i].x); e[i] = cvtpk(v[8 + 2 * i].y, v[9 + 2 * i].y); }
          else { b[i] = 0u; e[i] = 0u; }
        }
        LAS char* w = base + lane * 32;
        *(LAS u32x4*)(w + sw) = a; *(LAS u32x4*)(w + (sw ^ 16)) = b;
        *(LAS u32x4*)(w + 2048 + sw) = d; *(LAS u32x4*)(w + 2048 + (sw ^ 16)) = e;
      };
      f32x2 Lp[16], P[16], zb[9];
      Lp[0] = c; P[0] = (f32x2){1.f, 0.f}; P[1] = y1; zb[1] = y256;
      cmul2(Lp[1], Lp[0], z16, zb[2], zb[1], y256);
#pragma unroll
      for (int i = 2; i < 8; ++i) cmul3(Lp[i], Lp[i - 1], z16, P[i], P[i - 1], y1, zb[i + 1], zb[i], y256);
#pragma unroll
      for (int i = 8; i < 16; ++i) cmul2(Lp[i], Lp[i - 1], z16, P[i], P[i - 1], y1);
      STAMP_AT(12);
      put(s0, Lp, true);
      put(s1, P, true);
      STAMP_AT(13);
      const int rdo = (4 * q + (l16 >> 2)) * 32 + ((((lane & 3) >> 1) ^ (q & 1)) * 16) + (lane & 1) * 8;
      bf16x8 Lf[4];
#pragma unroll
      for (int s = 0; s < 4; ++s) Lf[s] = cat8(trread(s0 + rdo + s * 1024), trread(s0 + rdo + s * 1024 + 512));
      auto mm = [&]() {
        f32x4 acc = {0.f, 0.f, 0.f, 0.f};
#pragma unroll
        for (int s = 0; s < 4; ++s) {
          const bf16x8 hf = cat8(trread(s1 + rdo + s * 1024), trread(s1 + rdo + s * 1024 + 512));
          acc = mfma16(Lf[s], hf, acc);
        }
        return acc;
      };
      LAS float* kd = (LAS float*)(sm + K12_KD) + ((wid & 1) * 3 + (wid >> 1)) * KD_CH;
      if (lane < 16) kd[KD0 + lane] = 0.f;
      const f32x4 dA = mm();
      f32x2 v[16];
      v[0] = zb[1]; v[8] = zb[2]; v[12] = zb[3];
      cmul4(v[1], P[2], v[2], P[4], v[3], P[6], v[4], P[8], y256);
      cmul3(v[5], P[10], y256, v[6], P[12], y256, v[7], P[14], y256);
      cmul3(v[9], P[4], zb[2], v[10], P[8], zb[2], v[11], P[12], zb[2]);
      cmul3(v[13], P[4], zb[3], v[14], P[8], zb[3], v[15], P[12], zb[3]);
      put(s1, v, true);
      const f32x4 dB = mm();
#pragma unroll
      for (int i = 0; i < 4; ++i) v[2 * i] = zb[4 + i];
      cmul4(v[1], zb[4], v[3], zb[5], v[5], zb[6], v[7], zb[7], P[8]);
      v[8] = zb[8]; v[9] = (f32x2){0.f, 0.f};
      put(s1, v, false);
      const f32x4 dC = mm();
      STAMP_AT(15);
      const int h = l16;
      const int iB = h < 8 ? KD1 + h + 32 * q : KD2 + 64 * ((h - 8) >> 2) + (h & 3) + 16 * q, sB = h < 8 ? 8 : 4;
#pragma unroll
      for (int r = 0; r < 4; ++r) {
        kd[KD0 + 16 + 16 * (4 * q + r) + h] = dA[r];
        kd[iB + sB * r] = dB[r];
        if (h < 8) kd[KD3 + 32 * (h >> 1) + 2 * (4 * q + r) + (h & 1)] = dC[r];
      }
      if (q == 0) {
        if (h < 3) kd[KD0 + 16 + 256 + h + (h > 0)] = dB[0];
        if (h == 0) { kd[KD0 + 16 + 257] = dB[0]; kd[KD2 + 128] = dC[0]; kd[KD2 + 129] = dC[0]; }
        if (h == 8) { kd[KD1 + 128] = dB[0]; kd[KD1 + 129] = dB[0]; kd[KD3 + 128] = dC[0]; }
        if (h == 9) kd[KD1 + 130] = dB[0];
      }
    }
    STAMP_AT(2);
    {
      LAS u32* fl = (LAS u32*)(sm + K12_CNT);
      WAIT_L0();
      if (lane == 0) __hip_atomic_store(fl + wid, tag, __ATOMIC_RELAXED, __HIP_MEMORY_SCOPE_WORKGROUP);
      for (;;) {
        const u32 v = __hip_atomic_load(fl + min(lane, 5), __ATOMIC_RELAXED, __HIP_MEMORY_SCOPE_WORKGROUP);
        if (__all(v == tag)) break;
        __builtin_amdgcn_s_sleep(1);
      }
      asm volatile("" ::: "memory");
    }
    STAMP_AT(3);
    constexpr int HB = 3 * KD_CH;
    float s[3][11];
    int ch[3], M[3];
    bool act[3];
    auto win0 = [&](int T, int i) {
      act[i] = T < 102;
      ch[i] = min(T / 34, 2);
      const int t = min(T - ch[i] * 34, 33);
      M[i] = -8 + 8 * t;
      LAS float* kd = (LAS float*)(sm + K12_KD) + ch[i] * KD_CH + KD0 + 8 * t;
      const f32x4 u = *(LAS f32x4*)kd + *(LAS f32x4*)(kd + HB), v = *(LAS f32x4*)(kd + 4) + *(LAS f32x4*)(kd + HB + 4),
                  w = *(LAS f32x4*)(kd + 8) + *(LAS f32x4*)(kd + HB + 8);
      s[i][0] = w[3]; s[i][1] = w[2]; s[i][2] = w[1]; s[i][3] = w[0]; s[i][4] = v[3]; s[i][5] = v[2]; s[i][6] = v[1]; s[i][7] = v[0];
      s[i][8] = u[3]; s[i][9] = u[2]; s[i][10] = u[1];
    };
    {
      ch[0] = wid >> 1; act[0] = true;
      const int t = (wid & 1) * 64 + lane;
      M[0] = 1032 + 8 * t;
      LAS float* kd = (LAS float*)(sm + K12_KD) + ch[0] * KD_CH + KD3 + t;
      const float b = kd[0] + kd[HB], a = kd[1] + kd[HB + 1];
#pragma unroll
      for (int i = 0; i < 7; ++i) s[0][i] = a;
      s[0][7] = (t & 1) ? a : b;
      s[0][8] = s[0][9] = s[0][10] = b;
    }
    if (wid < 3) {
      ch[1] = wid; act[1] = true;
      M[1] = 520 + 8 * lane;
      LAS float* kd = (LAS float*)(sm + K12_KD) + wid * KD_CH + KD2 + 2 * lane;
      const f32x2 u = *(LAS f32x2*)kd + *(LAS f32x2*)(kd + HB), v = *(LAS f32x2*)(kd + 2) + *(LAS f32x2*)(kd + HB + 2);
      s[1][0] = v.y; s[1][1] = s[1][2] = s[1][3] = s[1][4] = s[1][5] = v.x; s[1][6] = s[1][7] = s[1][8] = u.y; s[1][9] = s[1][10] = u.x;
    } else if (wid < 5) {
      const int T = (wid - 3) * 64 + lane;
      act[1] = T < 96;
      ch[1] = min(T >> 5, 2);
      const int t = T & 31;
      M[1] = 264 + 8 * t;
      LAS float* kd = (LAS float*)(sm + K12_KD) + ch[1] * KD_CH + KD1 + 4 * t;
      const f32x4 u = *(LAS f32x4*)kd + *(LAS f32x4*)(kd + HB), v = *(LAS f32x4*)(kd + 4) + *(LAS f32x4*)(kd + HB + 4);
      s[1][0] = v[2]; s[1][1] = v[1]; s[1][2] = s[1][3] = s[1][4] = v[0]; s[1][5] = u[3]; s[1][6] = s[1][7] = s[1][8] = u[2]; s[1][9] = u[1]; s[1][10] = u[0];
    } else {
      win0(lane, 1);
    }
    act[2] = false;
    if (wid == 0) win0(64 + lane, 2);
#pragma unroll
    for (int i = 0; i < 3; ++i) {
      if (i == 2 && wid != 0) break;
      if (act[i]) {
        u32 E[5], O[5];
#pragma unroll
        for (int k = 0; k < 5; ++k) { E[k] = cvtpk(s[i][2 * k], s[i][2 * k + 1]); O[k] = cvtpk(s[i][2 * k + 1], s[i][2 * k + 2]); }
        LAS char* dst = sm + K12_KC + ch[i] * KC_CH + 2 * (2048 - M[i]);
        *(LAS u32x4*)(dst + 3 * KC_COPY) = (u32x4){E[0], E[1], E[2], E[3]};
        *(LAS u32x4*)(dst + 2 * KC_COPY) = (u32x4){O[0], O[1], O[2], O[3]};
        *(LAS u32x4*)(dst + 1 * KC_COPY) = (u32x4){E[1], E[2], E[3], E[4]};
        *(LAS u32x4*)(dst + 0 * KC_COPY) = (u32x4){O[1], O[2], O[3], O[4]};
      }
    }
  }
  STAMP_AT(4);
  u64 g[4];
#pragma unroll
  for (int k = 0; k < 4; ++k) g[k] = __hip_atomic_load((GAS u64*)(gran + 4 * tid + k), RLX_AGENT);
  __syncthreads();
  STAMP_AT(5);

  u16 dsk[3];
#pragma unroll
  for (int k = 0; k < 3; ++k) dsk[k] = Dsk[c0 + k];
  if (tid < 384) *(LAS u32*)(sm + K12_US + (tid >> 7) * 4608 + (tid & 127) * 4) = 0u;
  {
    u32 spins = 0;
    for (;;) {
      bool ok = true;
#pragma unroll
      for (int k = 0; k < 4; ++k) ok &= (u32)(g[k] >> 32) == tag;
      if (__all(ok)) break;
      if (++spins > (1u << 14)) {
#pragma unroll 1
        for (int k = 0; k < 4; ++k) {
          if ((u32)(g[k] >> 32) != tag) {
            const u16* p = x + (4 * tid + k) * D_;
            float s = 0.f;
            for (int i = 0; i < D_; ++i) s += bf2f(p[i]);
            const float mean = s * (1.f / D_);
            float qs = 0.f;
            for (int i = 0; i < D_; ++i) { const float d = bf2f(p[i]) - mean; qs = __builtin_fmaf(d, d, qs); }
            const u64 v = ((u64)tag << 32) | ln_pack(mean, qs * (1.f / D_));
            if (k == 0) g[0] = v; else if (k == 1) g[1] = v; else if (k == 2) g[2] = v; else g[3] = v;
          }
        }
        break;
      }
      __builtin_amdgcn_s_sleep(1);
#pragma unroll
      for (int k = 0; k < 4; ++k) g[k] = __hip_atomic_load((GAS u64*)(gran + 4 * tid + k), RLX_AGENT);
    }
    float m[4], r[4];
#pragma unroll
    for (int k = 0; k < 4; ++k) { m[k] = bflo((u32)g[k]); r[k] = __builtin_amdgcn_rcpf(bfhi((u32)g[k])); }
#pragma unroll
    for (int c = 0; c < 3; ++c) {
      const u32x2 px = *(LAS u32x2*)(sm + K12_XS + c * 4096 + tid * 8);
      u32 o[2];
#pragma unroll
      for (int p = 0; p < 2; ++p) {
        const u32 e = px[p];
        const u32 d = cvtpk(bflo(e) - m[2 * p], bfhi(e) - m[2 * p + 1]);
        o[p] = cvtpk(bflo(d) * r[2 * p], bfhi(d) * r[2 * p + 1]);
      }
      *(LAS u32x2*)(sm + K12_US + c * 4608 + 512 + tid * 8) = (u32x2){o[0], o[1]};
    }
  }
  STAMP_AT(6);
  __syncthreads();
  STAMP_AT(7);
  if (bid == 0 && tid == 0) __hip_atomic_store((GAS u32*)epoch, tag, RLX_AGENT);
  if (wid >= 4) {
    if (wid >= 6) pack_wd();
    __syncthreads();
    STAMP_FLUSH();
    return;
  }

  f32x4 accA[8], accB[8];
#pragma unroll
  for (int t = 0; t < 8; ++t) { accA[t] = (f32x4){0.f, 0.f, 0.f, 0.f}; accB[t] = accA[t]; }
  {
    const int kap = l16 & 3;
    const int kb0 = K12_KC + kap * KC_COPY + 2 * (2048 - 16 - l16 + 8 * q + kap) - 512 * 7;
    const int ub0 = K12_US + 480 + 32 * l16 + 16 * q;
    const int n1 = wid == 1 ? 1 : (wid == 2 ? 2 : 3);
    bf16x4 A0[8][2], A1[8][2];
    bf16x8 B0[8], B1[8];
    auto adr = [&](int un, LAS char*& kb, LAS char*& ub) {
      const int U = 6 * wid + min(un, 5), ch = U >> 3, r = (U & 7) * 2;
      kb = sm + kb0 + ch * KC_CH - 32 * r;
      ub = sm + ub0 + ch * 4608 - 32 * r;
    };
    auto rd = [&](int m, LAS char* kb, LAS char* ub, bf16x4 (&A)[8][2], bf16x8 (&B)[8]) {
      const int i = m / 3, k = m - 3 * i;
      if (k == 0) B[i] = *(LAS bf16x8*)(ub + 512 * i);
      else A[i][k - 1] = *(LAS bf16x4*)(kb + 512 * (7 - i) + 8 * (k - 1));
    };
    auto unit = [&](f32x4 (&acc)[8], const bf16x4 (&A)[8][2], const bf16x8 (&B)[8], bf16x4 (&An)[8][2], bf16x8 (&Bn)[8], LAS char* kb, LAS char* ub) {
      int m = 0;
#pragma unroll
      for (int d = 0; d < 8; ++d)
#pragma unroll
        for (int e = 0; e <= 2 * d; ++e) {
          const int i = e <= d ? d : 2 * d - e, j = e <= d ? e : d;
          if (i + j <= 7) {
            acc[i + j] = mfma16(cat8(A[i][0], A[i][1]), B[j], acc[i + j]);
            if (m < 24) rd(m, kb, ub, An, Bn);
            ++m;
            PIN();
          }
        }
    };
    auto pair = [&](f32x4 (&acc)[8], int it) {
      LAS char *kb, *ub;
      adr(2 * it + 1, kb, ub);
      WAIT_L0();
      unit(acc, A0, B0, A1, B1, kb, ub);
      adr(2 * it + 2, kb, ub);
      WAIT_L0();
      unit(acc, A1, B1, A0, B0, kb, ub);
    };
    {
      LAS char *kb, *ub;
      adr(0, kb, ub);
#pragma unroll
      for (int m = 0; m < 24; ++m) rd(m, kb, ub, A0, B0);
    }
#pragma unroll 1
    for (int it = 0; it < n1; ++it) pair(accA, it);
#pragma unroll 1
    for (int it = n1; it < 3; ++it) pair(accB, it);
  }
  STAMP_AT(8);
  {
    LAS char* pp = sm + K12_SCR + lane * 16;
    if (wid == 1 || wid == 2) {
#pragma unroll
      for (int t = 0; t < 8; ++t) *(LAS f32x4*)(pp + (wid - 1) * 8192 + t * 1024) = accA[t];
    }
    if (wid == 2) {
#pragma unroll
      for (int t = 0; t < 8; ++t) *(LAS f32x4*)(pp + 2 * 8192 + t * 1024) = accB[t];
    }
  }
  __syncthreads();
  STAMP_AT(9);
  if (wid != 2) {
    const int ch = wid == 3 ? 2 : wid;
    const float dk = bf2f(ch == 0 ? dsk[0] : (ch == 1 ? dsk[1] : dsk[2]));
    LAS char* pp = sm + K12_SCR + ch * 8192 + lane * 16;
    const srd_t so = mk_srd(hT, D_ * L_ * 2);
    const u32 dst = (u32)((c0 + ch) * L_ + 16 * l16 + 4 * q) * 2u;
#pragma unroll
    for (int t = 0; t < 8; ++t) {
      const f32x4 y = (wid == 1 ? accB[t] : accA[t]) + *(LAS f32x4*)(pp + t * 1024);
      const u32x2 xx = *(LAS u32x2*)(sm + K12_XS + ch * 4096 + 2 * (256 * t + 16 * l16 + 4 * q));
      const u32x2 uu = *(LAS u32x2*)(sm + K12_US + ch * 4608 + 512 + 2 * (256 * t + 16 * l16 + 4 * q));
      const float h0 = bflo(xx[0]) + __builtin_fmaf(bflo(uu[0]), dk, y[0]);
      const float h1 = bfhi(xx[0]) + __builtin_fmaf(bfhi(uu[0]), dk, y[1]);
      const float h2 = bflo(xx[1]) + __builtin_fmaf(bflo(uu[1]), dk, y[2]);
      const float h3 = bfhi(xx[1]) + __builtin_fmaf(bfhi(uu[1]), dk, y[3]);
      __builtin_amdgcn_raw_buffer_store_b64((u32x2){cvtpk(h0, h1), cvtpk(h2, h3)}, so, dst + 512 * t, 0, HT_AUX);
    }
  }
  STAMP_AT(10);
  STAMP_FLUSH();
}

constexpr int G1_H = 14336, G1_STAGE = 22528, G1_NST = 6, G1_NK = 20;
constexpr int G1_ST = G1_NST * G1_STAGE, G1_CS = G1_ST + 512;
constexpr int G1_LDS = G1_CS + 224 * 4;

template <int I> struct ic_t { static constexpr int value = I; };
template <int N, int I = 0, class F> DI void static_for(F&& f) {
  if constexpr (I < N) { f(ic_t<I>{}); static_for<N, I + 1>(f); }
}
template <int N> DI void wait_vm() { asm volatile("s_waitcnt vmcnt(%0)" ::"n"(N) : "memory"); }

constexpr int g1_first(int i) { return i < 0 ? 0 : (i == 0 ? 3 : (i == 1 ? 4 : (i == 2 ? 6 : (i == 3 ? 8 : i + 5)))); }
constexpr int g1_end(int i) { const int e = g1_first(i + 1); return e < G1_NK ? e : G1_NK; }
constexpr int g1_younger(int stage, int last, int P) {
  int n = 0;
  bool seen = false;
  for (int i = -1; i <= last; ++i) {
    const int f = g1_first(i) < G1_NK ? g1_first(i) : G1_NK;
    for (int s2 = f; s2 < g1_end(i); ++s2) { if (seen) n += P; if (s2 == stage) seen = true; }
  }
  return n < 63 ? n : 63;
}

template <bool SIX>
DI void g1_loader(const u16* __restrict__ hT, const char* __restrict__ wp1, const char* __restrict__ wp2, const float* __restrict__ csp,
                  LAS char* sm, const int lw, const int lane, const int fs, const int m0 STAMP_ARG2) {
  constexpr int P = SIX ? 6 : 5;
  const u32 lds0 = (u32)(size_t)sm;
  const srd_t srdW = mk_srd(wp1 + (size_t)fs * 10 * 28672, 10 * 28672);
  const srd_t srdH = mk_srd(hT + m0, (u32)(D_ * L_ - m0) * 2u);
  const u32 voW = lane * 16;
  const int hr = lane >> 4;
  const u32 voH = hr * (L_ * 2) + (((lane & 15) ^ (2 * (4 * (lw & 1) + hr))) * 16);
  const u32 pW = 1024 * lw, h0 = SIX ? lw + 2 : lw - 2;
  u32 lb = lds0, soW = pW, soH = h0 * (4 * L_ * 2);
  auto stage = [&]() {
#pragma unroll
    for (int i = 0; i < (SIX ? 4 : 3); ++i) dma16(lb + pW + 4096 * i, voW, srdW, soW + 4096 * i);
#pragma unroll
    for (int i = 0; i < 2; ++i) dma16(lb + G1_H + 1024 * (h0 + 4 * i), voH, srdH, soH + i * (16 * L_ * 2));
    soW += 14336; soH += 32 * L_ * 2;
    lb = lb == lds0 + (G1_NST - 1) * G1_STAGE ? lds0 : lb + G1_STAGE;
  };
  float cp[10];
  u32 sink = 0;
  constexpr int G1_TAIL = 12;
#pragma unroll
  for (int s2 = g1_first(-1); s2 < g1_end(-1); ++s2) stage();
  wait_vm<g1_younger(0, -1, P)>();
  BAR();
  STAMP_AT(1);
  auto interval = [&](auto ic) {
    constexpr int i = decltype(ic)::value;
#pragma unroll
    for (int s2 = g1_first(i); s2 < g1_end(i); ++s2) stage();
    if (i == G1_NK - 5) {
      const int c = min(64 * lw + lane, 223);
#pragma unroll
      for (int k = 0; k < 10; ++k) cp[k] = csp[(fs * 10 + k) * 224 + c];
      const int jj = blockIdx.x >> 3, fg = (blockIdx.x & 7) >> 1;
      const srd_t srdP = mk_srd(wp2 + (size_t)(fg & 1) * 4 * 276480, 4 * 276480);
      touch(sink, (u32)(jj * 270 + 64 * lw + lane) * 128u, srdP, 0u);
      touch(sink, (u32)(jj * 270 + min(256 + 64 * lw + lane, 269)) * 128u, srdP, 0u);
    }
    if (i + 1 < G1_NK) wait_vm<(g1_younger(i + 1, i, P) + (i >= G1_NK - 5 ? G1_TAIL : 0) < 63 ? g1_younger(i + 1, i, P) + (i >= G1_NK - 5 ? G1_TAIL : 0) : 63)>();
    BAR();
  };
  static_for<G1_NK>(interval);
  touch_done(sink);
  float cs = 0.f;
#pragma unroll
  for (int i = 0; i < 10; ++i) cs += cp[i];
  if (64 * lw + lane < 224) *(LAS float*)(sm + G1_CS + (64 * lw + lane) * 4) = cs;
  __syncthreads();
}

__global__ __launch_bounds__(512, 2) void gemm1(const u16* __restrict__ hT, const char* __restrict__ wp1, const char* __restrict__ wp2,
                                                const float* __restrict__ csp, char* __restrict__ Hp, float* __restrict__ r2 STAMP_ARG) {
  STAMP_DECL();
  STAMP_AT(0);
  extern __shared__ __attribute__((aligned(16))) char smem[];
  LAS char* sm = (LAS char*)smem;
  const u32 lds0 = (u32)(size_t)sm;
  const int tid = threadIdx.x, lane = tid & 63;
  const int w = rfl(tid >> 6);
  const int bid = blockIdx.x, xcd = bid & 7, jj = bid >> 3;
  const int mt = (xcd & 1) * 8 + (jj & 7), fs = (xcd >> 1) * 4 + (jj >> 3);
  const int m0 = mt * 128;

  if (w >= 4) {
    if (w < 6) g1_loader<true>(hT, wp1, wp2, csp, sm, w - 4, lane, fs, m0 STAMP_FWD);
    else g1_loader<false>(hT, wp1, wp2, csp, sm, w - 4, lane, fs, m0 STAMP_FWD);
    STAMP_FLUSH();
    return;
  }

  const int mh = w >> 1, nh = w & 1;
  const int q = lane >> 4, qq = (lane & 15) >> 2, p = lane & 3;
  u32 aW = lds0 + nh * 7168 + lane * 16;
  u32 aH[4];
#pragma unroll
  for (int t = 0; t < 4; ++t) {
    const int tt = 4 * mh + ((t + 2 * nh) & 3);
    const int X = 2 * (4 * (q & 1) + qq);
    aH[t] = lds0 + G1_H + (4 * q + qq) * 256 + (((2 * tt) ^ X) + (p >> 1)) * 16 + 8 * (p & 1);
  }
  f32x4 acc[4][7], accS[2];
#pragma unroll
  for (int t = 0; t < 4; ++t)
#pragma unroll
    for (int j = 0; j < 7; ++j) acc[t][j] = (f32x4){0.f, 0.f, 0.f, 0.f};
  accS[0] = acc[0][0]; accS[1] = acc[0][0];
  u32x4 onesv = {0x3F803F80u, 0x3F803F80u, 0x3F803F80u, 0x3F803F80u};
  asm volatile("" : "+v"(onesv));
  const bf16x8 ones = __builtin_bit_cast(bf16x8, onesv);
  bf16x8 W0[7], W1[7];
  bf16x4 H0[4][2], H1[4][2];
  auto rd = [&](int m, bf16x8 (&W)[7], bf16x4 (&H)[4][2]) {
    if (m < 8) H[m >> 1][m & 1] = trread((LAS char*)(size_t)aH[m >> 1] + (m & 1) * 4096);
    else W[m - 8] = *(LAS bf16x8*)((LAS char*)(size_t)aW + (m - 8) * 1024);
  };
  const int sr = lane & 31;
  u32 sa = lds0 + G1_H + sr * 256 + ((fs ^ (2 * (sr & 7))) * 16) + w * 4, sv, svn, slo, shi;
  float a1 = 0.f, b1 = 0.f, a2 = 0.f, b2 = 0.f;
  auto step = [&](const bf16x8 (&W)[7], const bf16x4 (&H)[4][2], bf16x8 (&Wn)[7], bf16x4 (&Hn)[4][2], u32 adv, const u32& s_cur, u32& s_nxt) {
#pragma unroll
    for (int m = 0; m < 30; ++m) {
      const int j = m >> 2, t = m & 3;
      if (m < 28) acc[t][j] = mfma16(W[j], cat8(H[t][0], H[t][1]), acc[t][j]);
      else accS[m - 28] = mfma16(ones, cat8(H[m - 28][0], H[m - 28][1]), accS[m - 28]);
      if (m < 15) rd(m, Wn, Hn);
      if (m == 15) s_nxt = *(LAS u32*)((LAS char*)(size_t)sa);
      if (m == 16) vadd_s(aW, adv);
      if (m >= 17 && m < 21) vadd_s(aH[m - 17], adv);
      if (m == 21) vadd_s(sa, adv);
      if (m == 22) vshl16(slo, s_cur);
      if (m == 23) vhi16(shi, s_cur);
      if (m == 24) vaddf(a1, slo);
      if (m == 25) vaddf(b1, shi);
      if (m == 26) vsq(a2, slo);
      if (m == 27) vsq(b2, shi);
      PIN();
    }
  };
  auto advance = [&](int ks) {
    return ((ks + 2) % G1_NST == 0) ? (u32)(-(G1_NST - 1) * G1_STAGE) : (u32)G1_STAGE;
  };
  BAR();
  STAMP_AT(2);
  WAIT_L0();
#pragma unroll
  for (int m = 0; m < 15; ++m) rd(m, W0, H0);
  sv = *(LAS u32*)((LAS char*)(size_t)sa);
  aW += G1_STAGE; sa += G1_STAGE;
#pragma unroll
  for (int t = 0; t < 4; ++t) aH[t] += G1_STAGE;
#pragma unroll 1
  for (int ks = 0; ks < G1_NK; ks += 2) {
    WAIT_L0();
    BAR();
    step(W0, H0, W1, H1, advance(ks), sv, svn);
    WAIT_L0();
    BAR();
    step(W1, H1, W0, H0, advance(ks + 1), svn, sv);
  }
  STAMP_AT(6);
  LAS float* st = (LAS float*)(sm + G1_ST);
  if (lane < 16) {
#pragma unroll
    for (int t = 0; t < 2; ++t) st[(4 * mh + ((t + 2 * nh) & 3)) * 16 + lane] = rbf(accS[t][0] * (1.f / D_));
  }
  __syncthreads();
  float mean[4];
#pragma unroll
  for (int t = 0; t < 4; ++t) mean[t] = st[(4 * mh + ((t + 2 * nh) & 3)) * 16 + (lane & 15)];
  const int g0 = ((27 * fs) >> 1) + 7 * nh;
  const srd_t so = mk_srd(Hp, 32 * 54 * 4096);
  f32x4 cv[7];
#pragma unroll
  for (int j = 0; j < 7; ++j) cv[j] = *(LAS f32x4*)(sm + G1_CS + ((7 * nh + j) * 16 + 4 * q) * 4);
#pragma unroll
  for (int pr = 0; pr < 2; ++pr) {
    const int tt = 4 * mh + ((2 * pr + (q & 1) + 2 * nh) & 3);
    const u32 base = (u32)((2 * mt + (tt >> 2)) * 54 * 4096 + (tt & 3) * 1024 + (lane & 15) * 16);
    u32 pk[7][2];
#pragma unroll
    for (int j = 0; j < 7; ++j) {
      const f32x4 a = __builtin_elementwise_fma(cv[j], (f32x4)(-mean[2 * pr]), acc[2 * pr][j]);
      const f32x4 b = __builtin_elementwise_fma(cv[j], (f32x4)(-mean[2 * pr + 1]), acc[2 * pr + 1][j]);
      f32x4 gv, uv;
#pragma unroll
      for (int r = 0; r < 4; ++r) {
        auto s = __builtin_amdgcn_permlane16_swap(__float_as_uint(a[r]), __float_as_uint(b[r]), false, false);
        gv[r] = __uint_as_float(s[0]); uv[r] = __uint_as_float(s[1]);
      }
      const f32x4 pv = gv * uv;
      pk[j][0] = cvtpk(pv[0], pv[1]); pk[j][1] = cvtpk(pv[2], pv[3]);
      if (j & 1) {
        auto s0 = __builtin_amdgcn_permlane32_swap(pk[j - 1][0], pk[j][0], false, false);
        auto s1x = __builtin_amdgcn_permlane32_swap(pk[j - 1][1], pk[j][1], false, false);
        const int gi = g0 + j - 1 + (q >> 1);
        __builtin_amdgcn_raw_buffer_store_b128((u32x4){s0[0], s1x[0], s0[1], s1x[1]}, so, base + (gi >> 2) * 4096 + (gi & 3) * 256, 0, H_AUX);
      }
    }
    const int gi = g0 + 6;
    __builtin_amdgcn_raw_buffer_store_b64((u32x2){pk[6][0], pk[6][1]}, so, base + (gi >> 2) * 4096 + (gi & 3) * 256 + 8 * (q >> 1), 0, H_AUX);
  }
  {
    a1 = dpp_add<0x128>(a1); b1 = dpp_add<0x128>(b1); a2 = dpp_add<0x128>(a2); b2 = dpp_add<0x128>(b2);
    a1 = dpp_add<0x124>(a1); b1 = dpp_add<0x124>(b1); a2 = dpp_add<0x124>(a2); b2 = dpp_add<0x124>(b2);
    a1 = dpp_add<0x122>(a1); b1 = dpp_add<0x122>(b1); a2 = dpp_add<0x122>(a2); b2 = dpp_add<0x122>(b2);
    a1 = dpp_add<0x121>(a1); b1 = dpp_add<0x121>(b1); a2 = dpp_add<0x121>(a2); b2 = dpp_add<0x121>(b2);
    float s1 = (lane & 1) ? b1 : a1, s2 = (lane & 1) ? b2 : a2;
    { auto r = __builtin_amdgcn_permlane16_swap(__float_as_uint(s1), __float_as_uint(s1), false, false); s1 = __uint_as_float(r[0]) + __uint_as_float(r[1]); }
    { auto r = __builtin_amdgcn_permlane16_swap(__float_as_uint(s2), __float_as_uint(s2), false, false); s2 = __uint_as_float(r[0]) + __uint_as_float(r[1]); }
    const float mn = s1 * (1.f / D_);
    const float var = fmaxf(s2 * (1.f / D_) - mn * mn, 0.f);
    const float rs = 1.f / bfhi(ln_pack(mn, var));
    if (lane < 2) r2[m0 + 8 * fs + 2 * w + lane] = rs * rs;
  }
  STAMP_AT(7);
  STAMP_FLUSH();
}

constexpr int G2_H = 10240, G2_STAGE = 18432, G2_NST = 8, G2_NK = 27, G2_RES = G2_NST * G2_STAGE;
constexpr int G2_LDS = G2_RES + 10240;

constexpr int g2_first(int j) { return j < 0 ? 0 : (j == 0 ? 4 : (j == 1 ? 5 : (j == 2 ? 9 : 2 * j + 5))); }
constexpr int g2_end(int j) { const int e = g2_first(j + 1); return e < G2_NK ? e : G2_NK; }
constexpr int G2_RESJ = 6;
constexpr int g2_younger(int tile, int last, int P) {
  int n = 0;
  bool seen = false;
  for (int j = -1; j <= last; ++j) {
    if (seen && j == G2_RESJ) n += 3;
    const int f = g2_first(j) < G2_NK ? g2_first(j) : G2_NK;
    for (int t = f; t < g2_end(j); ++t) { if (seen) n += P; if (t == tile) seen = true; }
  }
  return n < 63 ? n : 63;
}

template <bool ISW>
DI void g2_loader(const char* __restrict__ Hp, const char* __restrict__ wp2, const u16* __restrict__ hT, LAS char* sm, const int lw, const int lane,
                  const int mt, const int nt STAMP_ARG2) {
  constexpr int P = ISW ? 5 : 4;
  const u32 lds0 = (u32)(size_t)sm;
  const srd_t srd = ISW ? mk_srd(wp2 + (size_t)nt * 27 * 10240, 27 * 10240) : mk_srd(Hp + (size_t)mt * 54 * 4096, 54 * 4096);
  const u32 vo = lane * 16;
  const u32 l0 = lds0 + (ISW ? 5120 * lw : G2_H + 4096 * (lw - 2));
  u32 lb = l0, so = ISW ? 5120 * lw : 4096 * (lw - 2);
  auto tile = [&]() {
#pragma unroll
    for (int i = 0; i < P; ++i) dma16(lb + 1024 * i, vo, srd, so + 1024 * i);
    so += ISW ? 10240 : 8192;
    lb = lb == l0 + (G2_NST - 1) * G2_STAGE ? l0 : lb + G2_STAGE;
  };
#pragma unroll
  for (int t = g2_first(-1); t < g2_end(-1); ++t) tile();
  wait_vm<g2_younger(0, -1, P)>();
  BAR();
  STAMP_AT(1);
  auto interval = [&](auto jc) {
    constexpr int j = decltype(jc)::value;
    if (j == G2_RESJ) {
      const srd_t srdR = mk_srd(hT + (size_t)nt * 80 * L_ + mt * 64, (u32)((D_ - nt * 80) * L_ - mt * 64) * 2u);
#pragma unroll
      for (int i = 0; i < 3; ++i) {
        const int pc = min(lw + 4 * i, 9);
        dma16(lds0 + G2_RES + 1024 * pc, (u32)((8 * pc + (lane >> 3)) * (L_ * 2) + (lane & 7) * 16), srdR, 0u);
      }
    }
#pragma unroll
    for (int t = g2_first(j); t < g2_end(j); ++t) tile();
    constexpr int need = 2 * j + 2 < G2_NK ? 2 * j + 2 : G2_NK - 1;
    if (j >= 12) wait_vm<0>(); else wait_vm<g2_younger(need, j, P)>();
    BAR();
  };
  static_for<14>(interval);
  __syncthreads();
}

__global__ __launch_bounds__(512, 2) void gemm2(const char* __restrict__ Hp, const char* __restrict__ wp2, const u16* __restrict__ hT,
                                                const float* __restrict__ r2, u16* __restrict__ out STAMP_ARG) {
  STAMP_DECL();
  STAMP_AT(0);
  extern __shared__ __attribute__((aligned(16))) char smem[];
  LAS char* sm = (LAS char*)smem;
  const u32 lds0 = (u32)(size_t)sm;
  const int tid = threadIdx.x, lane = tid & 63;
  const int w = rfl(tid >> 6);
  const int bid = blockIdx.x, xcd = bid & 7, jj = bid >> 3;
  const int fg = xcd >> 1;
  const int mt = (xcd & 1) * 16 + (fg >> 1) * 8 + (jj & 7), nt = (fg & 1) * 4 + (jj >> 3);
  const int m0 = mt * 64, n0 = nt * 80;

  if (w >= 4) {
    if (w < 6) g2_loader<true>(Hp, wp2, hT, sm, w - 4, lane, mt, nt STAMP_FWD);
    else g2_loader<false>(Hp, wp2, hT, sm, w - 4, lane, mt, nt STAMP_FWD);
    STAMP_FLUSH();
    return;
  }

  const int th = w >> 1, kh = w & 1;
  u32 aW = lds0 + kh * 5120 + lane * 16, aH = lds0 + G2_H + kh * 4096 + th * 2048 + lane * 16;
  f32x4 acc[2][5];
#pragma unroll
  for (int t = 0; t < 2; ++t)
#pragma unroll
    for (int d = 0; d < 5; ++d) acc[t][d] = (f32x4){0.f, 0.f, 0.f, 0.f};
  const float rsc = r2[m0 + 16 * (2 * th + kh) + (lane & 15)];
  bf16x8 W0[5], W1[5], H0[2], H1[2];
  auto rd = [&](int m, bf16x8 (&W)[5], bf16x8 (&H)[2]) {
    if (m < 2) H[m] = *(LAS bf16x8*)((LAS char*)(size_t)aH + m * 1024);
    else W[m - 2] = *(LAS bf16x8*)((LAS char*)(size_t)aW + (m - 2) * 1024);
  };
  int rb = 0;
  auto next = [&]() {
    const u32 adv = rb == G2_NST - 1 ? (u32)(-(G2_NST - 1) * G2_STAGE) : (u32)G2_STAGE;
    rb = rb == G2_NST - 1 ? 0 : rb + 1;
    return adv;
  };
  auto step = [&](const bf16x8 (&W)[5], const bf16x8 (&H)[2], bf16x8 (&Wn)[5], bf16x8 (&Hn)[2]) {
    const u32 adv = next();
#pragma unroll
    for (int m = 0; m < 10; ++m) {
      acc[m & 1][m >> 1] = mfma16(W[m >> 1], H[m & 1], acc[m & 1][m >> 1]);
      if (m < 7) rd(m, Wn, Hn);
      if (m == 7) vadd_s(aW, adv);
      if (m == 8) vadd_s(aH, adv);
      PIN();
    }
  };
  BAR();
  STAMP_AT(2);
  WAIT_L0();
  {
#pragma unroll
    for (int m = 0; m < 7; ++m) rd(m, W0, H0);
    const u32 adv = next(); aW += adv; aH += adv;
  }
#pragma unroll 1
  for (int j = 0; j < 13; ++j) {
    WAIT_L0();
    BAR();
    step(W0, H0, W1, H1);
    WAIT_L0();
    step(W1, H1, W0, H0);
  }
  WAIT_L0();
  BAR();
#pragma unroll
  for (int m = 0; m < 10; ++m) acc[m & 1][m >> 1] = mfma16(W0[m >> 1], H0[m & 1], acc[m & 1][m >> 1]);
  STAMP_AT(6);
  {
    LAS char* ex = sm + w * 5120 + lane * 16;
#pragma unroll
    for (int d = 0; d < 5; ++d) *(LAS f32x4*)(ex + d * 1024) = kh ? acc[0][d] : acc[1][d];
  }
  __syncthreads();
  const int q = lane >> 4, qq = (lane & 15) >> 2, p = lane & 3;
  const int tt = 2 * th + kh;
  u16* orow = out + (size_t)(m0 + 16 * tt + (lane & 15)) * D_ + n0 + 4 * q;
#pragma unroll
  for (int d = 0; d < 5; ++d) {
    const f32x4 o = ((kh ? acc[1][d] : acc[0][d]) + *(LAS f32x4*)(sm + (w ^ 1) * 5120 + lane * 16 + d * 1024)) * rsc;
    const u32x2 hr = __builtin_bit_cast(u32x2, trread(sm + G2_RES + (16 * d + 4 * q + qq) * 128 + 32 * tt + 8 * p));
    const float o0 = bflo(hr[0]) + o[0], o1 = bfhi(hr[0]) + o[1], o2 = bflo(hr[1]) + o[2], o3 = bfhi(hr[1]) + o[3];
    *(u32x2*)(orow + 16 * d) = (u32x2){cvtpk(o0, o1), cvtpk(o2, o3)};
  }
  STAMP_AT(7);
  STAMP_FLUSH();
}

constexpr size_t WS_STAMP = 32u << 20;
constexpr size_t WS_EPOCH = 0, WS_GRAN = 4096, WS_R2 = WS_GRAN + 16384, WS_CS = WS_R2 + 8192, WS_HT = 262144, WS_H = WS_HT + (size_t)D_ * L_ * 2,
                 WS_WP1 = WS_H + (size_t)32 * 54 * 4096, WS_WP2 = WS_WP1 + (size_t)16 * 10 * 28672;

extern "C" void kernel_launch(void* const* d_in, const int* in_sizes, int n_in, void* d_out, int out_size, void* d_ws, size_t ws_size,
                              hipStream_t stream) {
  char* ws = (char*)d_ws;
  const u16* x = (const u16*)d_in[0];
  u16* hT = (u16*)(ws + WS_HT);
  float* csp = (float*)(ws + WS_CS);
  (void)hipFuncSetAttribute((const void*)k12, hipFuncAttributeMaxDynamicSharedMemorySize, K12_LDS);
  (void)hipFuncSetAttribute((const void*)gemm1, hipFuncAttributeMaxDynamicSharedMemorySize, G1_LDS);
  (void)hipFuncSetAttribute((const void*)gemm2, hipFuncAttributeMaxDynamicSharedMemorySize, G2_LDS);
  k12<<<256, 512, K12_LDS, stream>>>(x, (const u16*)d_in[1], (const u16*)d_in[2], (const u16*)d_in[3], (const u16*)d_in[4],
                                     (const u16*)d_in[5], (const u16*)d_in[6], (const u16*)d_in[7], (const u16*)d_in[8],
                                     (const u16*)d_in[10], (const u16*)d_in[11], (const u16*)d_in[12], hT, ws + WS_WP1, ws + WS_WP2, csp,
                                     (u64*)(ws + WS_GRAN), (u32*)(ws + WS_EPOCH) STAMP_PASS(0));
  gemm1<<<256, 512, G1_LDS, stream>>>(hT, ws + WS_WP1, ws + WS_WP2, csp, ws + WS_H, (float*)(ws + WS_R2) STAMP_PASS(1));
  gemm2<<<256, 512, G2_LDS, stream>>>(ws + WS_H, ws + WS_WP2, hT, (const float*)(ws + WS_R2), (u16*)d_out STAMP_PASS(2));
}
```

```cpp
#include <hip/hip_runtime.h>
#include <stdint.h>

typedef unsigned short u16;
typedef unsigned int u32;
typedef unsigned long long u64;
typedef float f32x2 __attribute__((ext_vector_type(2)));
typedef float f32x4 __attribute__((ext_vector_type(4)));
typedef __bf16 bf16x2 __attribute__((ext_vector_type(2)));
typedef __bf16 bf16x4 __attribute__((ext_vector_type(4)));
typedef __bf16 bf16x8 __attribute__((ext_vector_type(8)));
typedef u32 u32x2 __attribute__((ext_vector_type(2)));
typedef u32 u32x4 __attribute__((ext_vector_type(4)));
#define LAS __attribute__((address_space(3)))
#define GAS __attribute__((address_space(1)))
#define DI __device__ __forceinline__
#define RLX_AGENT __ATOMIC_RELAXED, __HIP_MEMORY_SCOPE_AGENT

#define STAMP_ARG
#define STAMP_PASS(k)
#define STAMP_DECL()
#define STAMP_FWD
#define STAMP_ARG2
#define STAMP_AT(i)
#define STAMP_FLUSH()
constexpr int L_ = 2048, D_ = 640, NS_ = 128, F_ = 1728;

DI float bflo(u32 v) { return __uint_as_float(v << 16); }
DI float bfhi(u32 v) { return __uint_as_float(v & 0xffff0000u); }
DI float bf2f(u16 v) { return __uint_as_float((u32)v << 16); }
DI u32 cvtpk(float lo, float hi) {
  f32x2 v = {lo, hi};
  bf16x2 b = __builtin_convertvector(v, bf16x2);
  return __builtin_bit_cast(u32, b);
}
DI float rbf(float x) { return bflo(cvtpk(x, x)); }
DI u32 rfl(u32 x) { return __builtin_amdgcn_readfirstlane(x); }

template <int CTRL>
DI float dpp_add(float v) {
  int t = __builtin_amdgcn_update_dpp(0, __float_as_int(v), CTRL, 0xf, 0xf, true);
  return v + __int_as_float(t);
}
#define CM_MUL(r, a, b) "v_pk_mul_f32 " r ", " a ", " b " op_sel:[0,0] op_sel_hi:[0,1]\n\t"
#define CM_FMA(r, a, b) "v_pk_fma_f32 " r ", " a ", " b ", " r " op_sel:[1,1,0] op_sel_hi:[1,0,1] neg_lo:[0,1,0]\n\t"
DI void cmul2(f32x2& r0, f32x2 a0, f32x2 b0, f32x2& r1, f32x2 a1, f32x2 b1) {
  asm(CM_MUL("%0", "%2", "%3") CM_MUL("%1", "%4", "%5") CM_FMA("%0", "%2", "%3") CM_FMA("%1", "%4", "%5")
      : "=&v"(r0), "=&v"(r1) : "v"(a0), "v"(b0), "v"(a1), "v"(b1));
}
DI void cmul3(f32x2& r0, f32x2 a0, f32x2 b0, f32x2& r1, f32x2 a1, f32x2 b1, f32x2& r2, f32x2 a2, f32x2 b2) {
  asm(CM_MUL("%0", "%3", "%4") CM_MUL("%1", "%5", "%6") CM_MUL("%2", "%7", "%8") CM_FMA("%0", "%3", "%4") CM_FMA("%1", "%5", "%6") CM_FMA("%2", "%7", "%8")
      : "=&v"(r0), "=&v"(r1), "=&v"(r2) : "v"(a0), "v"(b0), "v"(a1), "v"(b1), "v"(a2), "v"(b2));
}
DI void cmul4(f32x2& r0, f32x2 a0, f32x2& r1, f32x2 a1, f32x2& r2, f32x2 a2, f32x2& r3, f32x2 a3, f32x2 b) {
  asm(CM_MUL("%0", "%4", "%8") CM_MUL("%1", "%5", "%8") CM_MUL("%2", "%6", "%8") CM_MUL("%3", "%7", "%8")
      CM_FMA("%0", "%4", "%8") CM_FMA("%1", "%5", "%8") CM_FMA("%2", "%6", "%8") CM_FMA("%3", "%7", "%8")
      : "=&v"(r0), "=&v"(r1), "=&v"(r2), "=&v"(r3) : "v"(a0), "v"(a1), "v"(a2), "v"(a3), "v"(b));
}
DI f32x2 zpow(float ar, float ai, float p) {
  const float er = __builtin_amdgcn_exp2f(ar * p * 1.44269504088896341f);
  const float t = ai * p;
  const float CH = 0.15915494309189535f, CL = 6.4206383e-09f;
  const float hi = t * CH;
  float lo = __builtin_fmaf(t, CH, -hi);
  lo = __builtin_fmaf(t, CL, lo);
  const float fr = __builtin_amdgcn_fractf(hi) + lo;
  f32x2 r = {er * __builtin_amdgcn_cosf(fr), er * __builtin_amdgcn_sinf(fr)};
  return r;
}

DI bf16x8 cat8(bf16x4 a, bf16x4 b) { return __builtin_shufflevector(a, b, 0, 1, 2, 3, 4, 5, 6, 7); }
DI bf16x4 trread(LAS char* p) { return __builtin_amdgcn_ds_read_tr16_b64_v4bf16((LAS bf16x4*)p); }
DI f32x4 mfma16(bf16x8 a, bf16x8 b, f32x4 c) { return __builtin_amdgcn_mfma_f32_16x16x32_bf16(a, b, c, 0, 0, 0); }

DI u32 ln_pack(float mean, float var) {
  const float vb = rbf(var);
  const float t = rbf(vb + 1e-5f);
  const float s = __builtin_sqrtf(t);
  return cvtpk(mean, s);
}

typedef __amdgpu_buffer_rsrc_t srd_t;
DI srd_t mk_srd(const void* p, u32 bytes) { return __builtin_amdgcn_make_buffer_rsrc((void*)p, (short)0, (int)bytes, 0x00020000); }
DI void dma16(u32 lds_addr, u32 voff, srd_t srd, u32 soff) {
  asm volatile("s_mov_b32 m0, %0\n\ts_nop 0\n\tbuffer_load_dwordx4 %1, %2, %3 offen lds" ::"s"(lds_addr), "v"(voff), "s"(srd), "s"(soff) : "memory");
}
DI void touch(u32& sink, u32 voff, srd_t srd, u32 soff) {
  asm volatile("buffer_load_dword %0, %1, %2, %3 offen" : "+v"(sink) : "v"(voff), "s"(srd), "s"(soff) : "memory");
}
DI void touch_done(u32& sink) { asm volatile("s_waitcnt vmcnt(0)" : "+v"(sink)::"memory"); }
#define WAIT_V(n) asm volatile("s_waitcnt vmcnt(" #n ")" ::: "memory")
#define WAIT_L0() __builtin_amdgcn_s_waitcnt(0xC07F)
#define BAR() __builtin_amdgcn_s_barrier()
#define PIN() __builtin_amdgcn_sched_barrier(0)
DI void vadd_s(u32& a, u32 sadv) { asm volatile("v_add_u32 %0, %1, %0" : "+v"(a) : "s"(sadv)); }
DI void vshl16(u32& d, u32 x) { asm volatile("v_lshlrev_b32 %0, 16, %1" : "=v"(d) : "v"(x)); }
DI void vhi16(u32& d, u32 x) { asm volatile("v_and_b32 %0, 0xffff0000, %1" : "=v"(d) : "v"(x)); }
DI void vaddf(float& a, u32 x) { asm volatile("v_add_f32 %0, %1, %0" : "+v"(a) : "v"(x)); }
DI void vsq(float& a, u32 x) { asm volatile("v_fmac_f32 %0, %1, %1" : "+v"(a) : "v"(x)); }
#define PACK_AUX 16
#define HT_AUX 16
#define H_AUX 16

constexpr int KC_COPY = 4160;
constexpr int KC_CH = 4 * KC_COPY;
constexpr int K12_KC = 0;
constexpr int K12_US = 49920;
constexpr int K12_XS = K12_US + 3 * 4608;
constexpr int KD_CH = 684, KD0 = 0, KD1 = 288, KD2 = 420, KD3 = 552;
constexpr int K12_KD = K12_XS + 3 * 4096;
constexpr int K12_SCR = K12_KD + 6 * KD_CH * 4;
constexpr int K12_FLG = K12_SCR + 6 * 8192;
constexpr int K12_WD = K12_FLG + 32;
constexpr int K12_LDS = K12_WD + 2 * 5760;
constexpr int NCONV = 220;
constexpr int PK_U = 14352, PK_SLAB = 28928;

__global__ __launch_bounds__(512, 2) void k12(const u16* __restrict__ x, const u16* __restrict__ Are, const u16* __restrict__ Aim,
                                              const u16* __restrict__ Bre, const u16* __restrict__ Bim, const u16* __restrict__ Cre,
                                              const u16* __restrict__ Cim, const u16* __restrict__ logdt, const u16* __restrict__ Dsk,
                                              const u16* __restrict__ wg, const u16* __restrict__ wu, const u16* __restrict__ wd,
                                              u16* __restrict__ hT, char* __restrict__ wp1, char* __restrict__ wp2,
                                              float* __restrict__ csp, u64* gran, u32* epoch STAMP_ARG) {
  STAMP_DECL();
  STAMP_AT(0);
  extern __shared__ __attribute__((aligned(16))) char smem[];
  LAS char* sm = (LAS char*)smem;
  const u32 lds0 = (u32)(size_t)sm;
  const int tid = threadIdx.x, lane = tid & 63;
  const int wid = rfl(tid >> 6);
  const int bid = blockIdx.x, xcd = bid & 7, slot = bid >> 3;
  const int wq0 = (55 * xcd + 1) >> 1, ncv = ((55 * (xcd + 1) + 1) >> 1) - wq0;
  const int wq = wq0 + slot;
  const bool is_conv = slot < ncv;
  const int cg = wq / 22, cp = wq - 22 * cg;
  const int c0 = is_conv ? 64 * cg + (cp < 20 ? 3 * cp : 59 + 2 * (cp - 20)) : 0;
  const int q = lane >> 4, l16 = lane & 15;
  u32 pa[6], ldt = 0;
  if (is_conv && wid < 6) {
    const int o = (c0 + (wid >> 1)) * NS_ + (wid & 1) * 64 + lane;
    pa[0] = Are[o]; pa[1] = Aim[o]; pa[2] = Bre[o]; pa[3] = Bim[o]; pa[4] = Cre[o]; pa[5] = Cim[o];
    ldt = logdt[c0 + (wid >> 1)];
  }
  const int tok = bid * 8 + 4 * (wid & 1) + q;
  u32x2 xr[10];
  if (wid == 2 || wid == 3) {
    const u32x2* p = (const u32x2*)(x + tok * D_);
#pragma unroll
    for (int i = 0; i < 10; ++i) xr[i] = p[l16 + 16 * i];
  }
  u32 tagv;
  {
    u32 zero = 0;
    asm volatile("" : "+v"(zero));
    tagv = ((const GAS u32*)epoch)[zero];
  }
  const int e0 = min((c0 & ~1), D_ - 4), sh = c0 - e0;
  BAR();
  u32x2 xv[4][4];
  const int rot = tid - 384 + 23 * (wq % 22);
  if (is_conv && wid >= 6) {
#pragma unroll
    for (int g4 = 0; g4 < 4; ++g4)
#pragma unroll
      for (int k = 0; k < 4; ++k) xv[g4][k] = *(const u32x2*)(x + (4 * ((rot + 128 * g4) & 511) + k) * D_ + e0);
  }
  STAMP_AT(1);

  auto ln1 = [&](u32 tag) {
#pragma unroll
    for (int i = 0; i < 10; ++i) asm volatile("" : "+v"(xr[i]));
    const float piv = bflo(__builtin_amdgcn_update_dpp(0u, xr[0][0], 0x150, 0xf, 0xf, true));
    f32x2 s2 = {0.f, 0.f}, q2 = {0.f, 0.f};
    const f32x2 pv2 = {piv, piv};
#pragma unroll
    for (int i = 0; i < 10; ++i)
#pragma unroll
      for (int h = 0; h < 2; ++h) {
        const u32 e = xr[i][h];
        const f32x2 d = (f32x2){bflo(e), bfhi(e)} - pv2;
        s2 += d;
        q2 = __builtin_elementwise_fma(d, d, q2);
      }
    float s = s2.x + s2.y, qs = q2.x + q2.y;
    s = dpp_add<0x128>(s); qs = dpp_add<0x128>(qs);
    s = dpp_add<0x124>(s); qs = dpp_add<0x124>(qs);
    s = dpp_add<0x122>(s); qs = dpp_add<0x122>(qs);
    s = dpp_add<0x121>(s); qs = dpp_add<0x121>(qs);
    const float md = s * (1.f / D_);
    const float var = fmaxf(qs * (1.f / D_) - md * md, 0.f);
    const u32 st = ln_pack(piv + md, var);
    if (l16 == 0) __hip_atomic_store((GAS u64*)(gran + tok), ((u64)tag << 32) | st, RLX_AGENT);
  };
  auto get_tag = [&]() { asm volatile("" : "+v"(tagv)); return rfl(tagv) + 1u; };

  u32 tag = 0;
  if (wid == 2 || wid == 3) { tag = get_tag(); ln1(tag); }
  if (!is_conv) {
    const int sp = 32 * xcd - wq0 + (slot - ncv);
    const int sb = (sp * 160) / 36, nsl = ((sp + 1) * 160) / 36 - sb;
    const int p = lane & 3, qq = l16 >> 2;
    {
      const srd_t sg = mk_srd(wg, D_ * F_ * 2), su = mk_srd(wu, D_ * F_ * 2);
#pragma unroll
      for (int h = 0; h < 2; ++h) {
        const int pr = wid + 8 * h;
        if (pr < 2 * nsl) {
          const int s = sb + (pr >> 1), fs = s / 10, kt = s - 10 * fs, f0 = 8 * ((27 * fs) >> 1);
          const u32 so = (u32)(kt * 64 * F_ + f0) * 2u, lb = lds0 + (pr >> 1) * PK_SLAB + (pr & 1) * PK_U;
#pragma unroll
          for (int i = 0; i < 14; ++i) {
            const int S = 64 * i + lane, rho = S / 14, sgm = S - 14 * rho;
            dma16(lb + 1024 * i, (u32)(rho * (F_ * 2) + sgm * 16), (pr & 1) ? su : sg, so);
          }
        }
      }
    }
    WAIT_V(0);
    BAR();
    STAMP_AT(11);
    {
      const srd_t sp = mk_srd(wp1, 16 * 10 * 28672);
      const int rd = (p & 1) * PK_U + (4 * q + qq) * 224 + 8 * (p >> 1);
      const bf16x2 ones = {(__bf16)1.0f, (__bf16)1.0f};
#pragma unroll 1
      for (int job = wid; job < 14 * nsl; job += 8) {
        const int sl = job / 14, ct = job - 14 * sl;
        const int s = sb + sl, fs = s / 10, kt = s - 10 * fs;
        LAS char* b = sm + sl * PK_SLAB + rd + ct * 16;
        float cs = 0.f;
#pragma unroll
        for (int ks = 0; ks < 2; ++ks) {
          const bf16x8 wf = cat8(trread(b + ks * 7168), trread(b + ks * 7168 + 3584));
          const u32x4 wv = __builtin_bit_cast(u32x4, wf);
#pragma unroll
          for (int i = 0; i < 4; ++i) { const u32 e = wv[i]; cs = __builtin_amdgcn_fdot2_f32_bf16(__builtin_bit_cast(bf16x2, e), ones, cs, false); }
          __builtin_amdgcn_raw_buffer_store_b128(wv, sp, (u32)((((fs * 10 + kt) * 2 + ks) * 14 + ct) * 1024 + lane * 16), 0, PACK_AUX);
        }
        { auto r = __builtin_amdgcn_permlane16_swap(__float_as_uint(cs), __float_as_uint(cs), false, false); cs = __uint_as_float(r[0]) + __uint_as_float(r[1]); }
        { auto r = __builtin_amdgcn_permlane32_swap(__float_as_uint(cs), __float_as_uint(cs), false, false); cs = __uint_as_float(r[0]) + __uint_as_float(r[1]); }
        if (lane < 16) csp[(fs * 10 + kt) * 224 + ct * 16 + lane] = cs;
      }
    }
    STAMP_AT(15);
    STAMP_FLUSH();
    return;
  }

  u64 g[4];
  auto look = [&]() {
#pragma unroll
    for (int k = 0; k < 4; ++k) g[k] = __hip_atomic_load((GAS u64*)(gran + 4 * tid + k), RLX_AGENT);
  };
  if (wid >= 6) {
    look();
#pragma unroll
    for (int g4 = 0; g4 < 4; ++g4) {
      u32 lo[4], hi[4];
#pragma unroll
      for (int k = 0; k < 4; ++k) {
        lo[k] = sh ? __builtin_amdgcn_alignbit(xv[g4][k][1], xv[g4][k][0], 16) : xv[g4][k][0];
        hi[k] = sh ? (xv[g4][k][1] >> 16) : xv[g4][k][1];
      }
      LAS char* dst = sm + K12_XS + ((rot + 128 * g4) & 511) * 8;
      *(LAS u32x2*)(dst) = (u32x2){__builtin_amdgcn_perm(lo[1], lo[0], 0x05040100u), __builtin_amdgcn_perm(lo[3], lo[2], 0x05040100u)};
      *(LAS u32x2*)(dst + 4096) = (u32x2){__builtin_amdgcn_perm(lo[1], lo[0], 0x07060302u), __builtin_amdgcn_perm(lo[3], lo[2], 0x07060302u)};
      *(LAS u32x2*)(dst + 8192) = (u32x2){__builtin_amdgcn_perm(hi[1], hi[0], 0x05040100u), __builtin_amdgcn_perm(hi[3], hi[2], 0x05040100u)};
    }
    tag = get_tag();
  } else {
    {
      asm volatile("" : "+v"(ldt), "+v"(pa[0]), "+v"(pa[1]), "+v"(pa[2]), "+v"(pa[3]), "+v"(pa[4]), "+v"(pa[5]));
      STAMP_AT(14);
      const float dt = rbf(expf(bflo(ldt)));
      const float ar = dt * bflo(pa[0]), ai = dt * bflo(pa[1]);
      const float br = dt * bflo(pa[2]), bi = dt * bflo(pa[3]);
      const float cr = bflo(pa[4]), ci = bflo(pa[5]);
      const f32x2 c = {cr * br - ci * bi, cr * bi + ci * br};
      const f32x2 z16 = zpow(ar, ai, 16.f), y1 = zpow(ar, -ai, 1.f), y256 = zpow(ar, -ai, 256.f);
      LAS char* s0 = sm + K12_SCR + wid * 8192;
      LAS char* s1 = s0 + 4096;
      const int sw = ((lane >> 2) & 1) * 16;
      auto put = [&](LAS char* base, const f32x2 (&v)[16], bool both) {
        u32x4 a, b, d, e;
#pragma unroll
        for (int i = 0; i < 4; ++i) {
          a[i] = cvtpk(v[2 * i].x, v[2 * i + 1].x); d[i] = cvtpk(v[2 * i].y, v[2 * i + 1].y);
          if (both || i == 0) { b[i] = cvtpk(v[8 + 2 * i].x, v[9 + 2 * i].x); e[i] = cvtpk(v[8 + 2 * i].y, v[9 + 2 * i].y); }
          else { b[i] = 0u; e[i] = 0u; }
        }
        LAS char* w = base + lane * 32;
        *(LAS u32x4*)(w + sw) = a; *(LAS u32x4*)(w + (sw ^ 16)) = b;
        *(LAS u32x4*)(w + 2048 + sw) = d; *(LAS u32x4*)(w + 2048 + (sw ^ 16)) = e;
      };
      f32x2 Lp[16], P[16], zb[9];
      Lp[0] = c; P[0] = (f32x2){1.f, 0.f}; P[1] = y1; zb[1] = y256;
      cmul2(Lp[1], Lp[0], z16, zb[2], zb[1], y256);
#pragma unroll
      for (int i = 2; i < 8; ++i) cmul3(Lp[i], Lp[i - 1], z16, P[i], P[i - 1], y1, zb[i + 1], zb[i], y256);
#pragma unroll
      for (int i = 8; i < 16; ++i) cmul2(Lp[i], Lp[i - 1], z16, P[i], P[i - 1], y1);
      put(s0, Lp, true);
      put(s1, P, true);
      const int rdo = (4 * q + (l16 >> 2)) * 32 + ((((lane & 3) >> 1) ^ (q & 1)) * 16) + (lane & 1) * 8;
      bf16x8 Lf[4];
#pragma unroll
      for (int s = 0; s < 4; ++s) Lf[s] = cat8(trread(s0 + rdo + s * 1024), trread(s0 + rdo + s * 1024 + 512));
      auto mm = [&]() {
        f32x4 acc = {0.f, 0.f, 0.f, 0.f};
#pragma unroll
        for (int s = 0; s < 4; ++s) {
          const bf16x8 hf = cat8(trread(s1 + rdo + s * 1024), trread(s1 + rdo + s * 1024 + 512));
          acc = mfma16(Lf[s], hf, acc);
        }
        return acc;
      };
      LAS float* kd = (LAS float*)(sm + K12_KD) + ((wid & 1) * 3 + (wid >> 1)) * KD_CH;
      if (lane < 16) kd[KD0 + lane] = 0.f;
      const f32x4 dA = mm();
      f32x2 v[16];
      v[0] = zb[1]; v[8] = zb[2]; v[12] = zb[3];
      cmul4(v[1], P[2], v[2], P[4], v[3], P[6], v[4], P[8], y256);
      cmul3(v[5], P[10], y256, v[6], P[12], y256, v[7], P[14], y256);
      cmul3(v[9], P[4], zb[2], v[10], P[8], zb[2], v[11], P[12], zb[2]);
      cmul3(v[13], P[4], zb[3], v[14], P[8], zb[3], v[15], P[12], zb[3]);
      put(s1, v, true);
      const f32x4 dB = mm();
#pragma unroll
      for (int i = 0; i < 4; ++i) v[2 * i] = zb[4 + i];
      cmul4(v[1], zb[4], v[3], zb[5], v[5], zb[6], v[7], zb[7], P[8]);
      v[8] = zb[8]; v[9] = (f32x2){0.f, 0.f};
      put(s1, v, false);
      const f32x4 dC = mm();
      const int h = l16;
      const int iB = h < 8 ? KD1 + h + 32 * q : KD2 + 64 * ((h - 8) >> 2) + (h & 3) + 16 * q, sB = h < 8 ? 8 : 4;
#pragma unroll
      for (int r = 0; r < 4; ++r) {
        kd[KD0 + 16 + 16 * (4 * q + r) + h] = dA[r];
        kd[iB + sB * r] = dB[r];
        if (h < 8) kd[KD3 + 32 * (h >> 1) + 2 * (4 * q + r) + (h & 1)] = dC[r];
      }
      if (q == 0) {
        if (h < 3) kd[KD0 + 16 + 256 + h + (h > 0)] = dB[0];
        if (h == 0) { kd[KD0 + 16 + 257] = dB[0]; kd[KD2 + 128] = dC[0]; kd[KD2 + 129] = dC[0]; }
        if (h == 8) { kd[KD1 + 128] = dB[0]; kd[KD1 + 129] = dB[0]; kd[KD3 + 128] = dC[0]; }
        if (h == 9) kd[KD1 + 130] = dB[0];
      }
    }
    STAMP_AT(2);
    if (wid != 2 && wid != 3) tag = get_tag();
    {
      LAS u32* fl = (LAS u32*)(sm + K12_FLG);
      WAIT_L0();
      if (lane == 0) __hip_atomic_store(fl + wid, tag, __ATOMIC_RELAXED, __HIP_MEMORY_SCOPE_WORKGROUP);
      for (;;) {
        const u32 v = __hip_atomic_load(fl + min(lane, 5), __ATOMIC_RELAXED, __HIP_MEMORY_SCOPE_WORKGROUP);
        if (__all(v == tag)) break;
        __builtin_amdgcn_s_sleep(1);
      }
      asm volatile("" ::: "memory");
    }
    STAMP_AT(3);
    look();
    constexpr int HB = 3 * KD_CH;
    float s[3][11];
    int ch[3], M[3];
    bool act[3];
    auto win0 = [&](int T, int i) {
      act[i] = T < 102;
      ch[i] = min(T / 34, 2);
      const int t = min(T - ch[i] * 34, 33);
      M[i] = -8 + 8 * t;
      LAS float* kd = (LAS float*)(sm + K12_KD) + ch[i] * KD_CH + KD0 + 8 * t;
      const f32x4 u = *(LAS f32x4*)kd + *(LAS f32x4*)(kd + HB), v = *(LAS f32x4*)(kd + 4) + *(LAS f32x4*)(kd + HB + 4),
                  w = *(LAS f32x4*)(kd + 8) + *(LAS f32x4*)(kd + HB + 8);
      s[i][0] = w[3]; s[i][1] = w[2]; s[i][2] = w[1]; s[i][3] = w[0]; s[i][4] = v[3]; s[i][5] = v[2]; s[i][6] = v[1]; s[i][7] = v[0];
      s[i][8] = u[3]; s[i][9] = u[2]; s[i][10] = u[1];
    };
    {
      ch[0] = wid >> 1; act[0] = true;
      const int t = (wid & 1) * 64 + lane;
      M[0] = 1032 + 8 * t;
      LAS float* kd = (LAS float*)(sm + K12_KD) + ch[0] * KD_CH + KD3 + t;
      const float b = kd[0] + kd[HB], a = kd[1] + kd[HB + 1];
#pragma unroll
      for (int i = 0; i < 7; ++i) s[0][i] = a;
      s[0][7] = (t & 1) ? a : b;
      s[0][8] = s[0][9] = s[0][10] = b;
    }
    if (wid < 3) {
      ch[1] = wid; act[1] = true;
      M[1] = 520 + 8 * lane;
      LAS float* kd = (LAS float*)(sm + K12_KD) + wid * KD_CH + KD2 + 2 * lane;
      const f32x2 u = *(LAS f32x2*)kd + *(LAS f32x2*)(kd + HB), v = *(LAS f32x2*)(kd + 2) + *(LAS f32x2*)(kd + HB + 2);
      s[1][0] = v.y; s[1][1] = s[1][2] = s[1][3] = s[1][4] = s[1][5] = v.x; s[1][6] = s[1][7] = s[1][8] = u.y; s[1][9] = s[1][10] = u.x;
    } else if (wid < 5) {
      const int T = (wid - 3) * 64 + lane;
      act[1] = T < 96;
      ch[1] = min(T >> 5, 2);
      const int t = T & 31;
      M[1] = 264 + 8 * t;
      LAS float* kd = (LAS float*)(sm + K12_KD) + ch[1] * KD_CH + KD1 + 4 * t;
      const f32x4 u = *(LAS f32x4*)kd + *(LAS f32x4*)(kd + HB), v = *(LAS f32x4*)(kd + 4) + *(LAS f32x4*)(kd + HB + 4);
      s[1][0] = v[2]; s[1][1] = v[1]; s[1][2] = s[1][3] = s[1][4] = v[0]; s[1][5] = u[3]; s[1][6] = s[1][7] = s[1][8] = u[2]; s[1][9] = u[1]; s[1][10] = u[0];
    } else {
      win0(lane, 1);
    }
    act[2] = false;
    if (wid == 0) win0(64 + lane, 2);
#pragma unroll
    for (int i = 0; i < 3; ++i) {
      if (i == 2 && wid != 0) break;
      if (act[i]) {
        u32 E[5], O[5];
#pragma unroll
        for (int k = 0; k < 5; ++k) { E[k] = cvtpk(s[i][2 * k], s[i][2 * k + 1]); O[k] = cvtpk(s[i][2 * k + 1], s[i][2 * k + 2]); }
        LAS char* dst = sm + K12_KC + ch[i] * KC_CH + 2 * (2048 - M[i]);
        *(LAS u32x4*)(dst + 3 * KC_COPY) = (u32x4){E[0], E[1], E[2], E[3]};
        *(LAS u32x4*)(dst + 2 * KC_COPY) = (u32x4){O[0], O[1], O[2], O[3]};
        *(LAS u32x4*)(dst + 1 * KC_COPY) = (u32x4){E[1], E[2], E[3], E[4]};
        *(LAS u32x4*)(dst + 0 * KC_COPY) = (u32x4){O[1], O[2], O[3], O[4]};
      }
    }
  }
  STAMP_AT(4);
  float dsk[3];
#pragma unroll
  for (int k = 0; k < 3; ++k) dsk[k] = bf2f(Dsk[c0 + k]);
  if (tid < 384) *(LAS u32*)(sm + K12_US + (tid >> 7) * 4608 + (tid & 127) * 4) = 0u;
  STAMP_AT(5);
  __syncthreads();
  STAMP_AT(6);
  float m[4], r[4];
  {
    u32 spins = 0;
    for (;;) {
      bool ok = true;
#pragma unroll
      for (int k = 0; k < 4; ++k) ok &= (u32)(g[k] >> 32) == tag;
      if (__all(ok)) break;
      if (++spins > (1u << 14)) {
#pragma unroll 1
        for (int k = 0; k < 4; ++k) {
          if ((u32)(g[k] >> 32) != tag) {
            const u16* p = x + (4 * tid + k) * D_;
            float s = 0.f;
            for (int i = 0; i < D_; ++i) s += bf2f(p[i]);
            const float mean = s * (1.f / D_);
            float qs = 0.f;
            for (int i = 0; i < D_; ++i) { const float d = bf2f(p[i]) - mean; qs = __builtin_fmaf(d, d, qs); }
            const u64 v = ((u64)tag << 32) | ln_pack(mean, qs * (1.f / D_));
            if (k == 0) g[0] = v; else if (k == 1) g[1] = v; else if (k == 2) g[2] = v; else g[3] = v;
          }
        }
        break;
      }
      __builtin_amdgcn_s_sleep(2);
      look();
    }
#pragma unroll
    for (int k = 0; k < 4; ++k) { m[k] = bflo((u32)g[k]); r[k] = __builtin_amdgcn_rcpf(bfhi((u32)g[k])); }
  }
#pragma unroll
  for (int c = 0; c < 3; ++c) {
    const u32x2 px = *(LAS u32x2*)(sm + K12_XS + c * 4096 + tid * 8);
    u32 o[2];
#pragma unroll
    for (int p = 0; p < 2; ++p) {
      const u32 e = px[p];
      const u32 d = cvtpk(bflo(e) - m[2 * p], bfhi(e) - m[2 * p + 1]);
      o[p] = cvtpk(bflo(d) * r[2 * p], bfhi(d) * r[2 * p + 1]);
    }
    *(LAS u32x2*)(sm + K12_US + c * 4608 + 512 + tid * 8) = (u32x2){o[0], o[1]};
  }
  f32x4 accA[8], accB[8];
  bf16x4 A0[8][2], A1[8][2];
  bf16x8 B0[8], B1[8];
  const int kap = l16 & 3;
  const int kb0 = K12_KC + kap * KC_COPY + 2 * (2048 - 16 - l16 + 8 * q + kap) - 512 * 7;
  const int ub0 = K12_US + 480 + 32 * l16 + 16 * q;
  auto adr = [&](int un, LAS char*& kb, LAS char*& ub) {
    const int U = 6 * (wid & 3) + min(un, 5), ch = U >> 3, rs = (U & 7) * 2;
    kb = sm + kb0 + ch * KC_CH - 32 * rs;
    ub = sm + ub0 + ch * 4608 - 32 * rs;
  };
  auto rd = [&](int mm, LAS char* kb, LAS char* ub, bf16x4 (&A)[8][2], bf16x8 (&B)[8]) {
    const int i = mm / 3, k = mm - 3 * i;
    if (k == 0) B[i] = *(LAS bf16x8*)(ub + 512 * i);
    else A[i][k - 1] = *(LAS bf16x4*)(kb + 512 * (7 - i) + 8 * (k - 1));
  };
  LAS char *kb1, *ub1;
  adr(0, kb1, ub1);
  if (wid < 4) {
#pragma unroll
    for (int t = 0; t < 8; ++t) { accA[t] = (f32x4){0.f, 0.f, 0.f, 0.f}; accB[t] = accA[t]; asm volatile("" : "+v"(accA[t]), "+v"(accB[t])); }
#pragma unroll
    for (int mm = 0; mm < 24; ++mm) if (mm % 3) rd(mm, kb1, ub1, A0, B0);
  }
  __syncthreads();
  STAMP_AT(7);
  if (bid == 0 && tid == 0) __hip_atomic_store((GAS u32*)epoch, tag, RLX_AGENT);
  if (wid >= 6 && wq < 216) {
    const int ks = wid - 6, nt = wq / 27, kt = wq - 27 * nt;
    const srd_t sd = mk_srd(wd, F_ * D_ * 2);
    const u32 so = (u32)((kt * 64 + ks * 32) * D_ + nt * 80) * 2u, lb = lds0 + K12_WD + ks * 5760;
    const int rho = lane / 10, sgm = lane - 10 * rho;
    if (lane < 60) {
#pragma unroll
      for (int i = 0; i < 6; ++i) {
        if (i < 5 || rho < 2) dma16(lb + 960 * i, (u32)((6 * i + rho) * (D_ * 2) + sgm * 16), sd, so);
      }
    }
    WAIT_V(0);
    const srd_t sp = mk_srd(wp2, 8 * 27 * 10240);
    const int p = lane & 3, qq = l16 >> 2;
    LAS char* b = sm + K12_WD + ks * 5760 + (8 * q + qq) * 160 + 8 * p;
#pragma unroll
    for (int dt = 0; dt < 5; ++dt) {
      const bf16x8 wf = cat8(trread(b + dt * 32), trread(b + 4 * 160 + dt * 32));
      __builtin_amdgcn_raw_buffer_store_b128(__builtin_bit_cast(u32x4, wf), sp, (u32)(((wq * 2 + ks) * 5 + dt) * 1024 + lane * 16), 0, PACK_AUX);
    }
  }
  if (wid < 4) {
#pragma unroll
    for (int mm = 0; mm < 24; mm += 3) rd(mm, kb1, ub1, A0, B0);
    const int n1 = wid == 1 ? 1 : (wid == 2 ? 2 : 3);
    auto unit = [&](f32x4 (&acc)[8], const bf16x4 (&A)[8][2], const bf16x8 (&B)[8], bf16x4 (&An)[8][2], bf16x8 (&Bn)[8], LAS char* kb, LAS char* ub) {
      int mm = 0;
#pragma unroll
      for (int i = 0; i < 8; ++i)
#pragma unroll
        for (int t = 7; t >= i; --t) {
          acc[t] = mfma16(cat8(A[i][0], A[i][1]), B[t - i], acc[t]);
          if (mm < 24) rd(mm, kb, ub, An, Bn);
          ++mm;
          PIN();
        }
    };
    auto pair = [&](f32x4 (&acc)[8], int it) {
      LAS char *kb, *ub;
      adr(2 * it + 1, kb, ub);
      WAIT_L0();
      unit(acc, A0, B0, A1, B1, kb, ub);
      adr(2 * it + 2, kb, ub);
      WAIT_L0();
      unit(acc, A1, B1, A0, B0, kb, ub);
    };
#pragma unroll 1
    for (int it = 0; it < n1; ++it) pair(accA, it);
#pragma unroll 1
    for (int it = n1; it < 3; ++it) pair(accB, it);
    STAMP_AT(8);
    LAS char* pp = sm + K12_SCR + lane * 16;
    const int sA = wid == 0 ? 0 : 2 * wid - 1;
#pragma unroll
    for (int t = 0; t < 8; ++t) *(LAS f32x4*)(pp + sA * 8192 + t * 1024) = accA[t];
    if (wid == 1 || wid == 2) {
#pragma unroll
      for (int t = 0; t < 8; ++t) *(LAS f32x4*)(pp + 2 * wid * 8192 + t * 1024) = accB[t];
    }
  }
  __syncthreads();
  STAMP_AT(9);
  {
    const srd_t so = mk_srd(hT, D_ * L_ * 2);
#pragma unroll
    for (int i = 0; i < 3; ++i) {
      const int pc = wid * 3 + i, ch = pc >> 3, t = pc & 7;
      const float dk = ch == 0 ? dsk[0] : (ch == 1 ? dsk[1] : dsk[2]);
      LAS char* pp = sm + K12_SCR + 2 * ch * 8192 + t * 1024 + lane * 16;
      const f32x4 y = *(LAS f32x4*)pp + *(LAS f32x4*)(pp + 8192);
      const int l0 = 256 * t + 16 * l16 + 4 * q;
      const u32x2 xx = *(LAS u32x2*)(sm + K12_XS + ch * 4096 + 2 * l0);
      const u32x2 uu = *(LAS u32x2*)(sm + K12_US + ch * 4608 + 512 + 2 * l0);
      const float h0 = bflo(xx[0]) + __builtin_fmaf(bflo(uu[0]), dk, y[0]);
      const float h1 = bfhi(xx[0]) + __builtin_fmaf(bfhi(uu[0]), dk, y[1]);
      const float h2 = bflo(xx[1]) + __builtin_fmaf(bflo(uu[1]), dk, y[2]);
      const float h3 = bfhi(xx[1]) + __builtin_fmaf(bfhi(uu[1]), dk, y[3]);
      __builtin_amdgcn_raw_buffer_store_b64((u32x2){cvtpk(h0, h1), cvtpk(h2, h3)}, so, (u32)((c0 + ch) * L_ + l0) * 2u, 0, HT_AUX);
    }
  }
  STAMP_AT(10);
  STAMP_FLUSH();
}

constexpr int G1_H = 14336, G1_STAGE = 22528, G1_NST = 6, G1_NK = 20;
constexpr int G1_ST = G1_NST * G1_STAGE, G1_CS = G1_ST + 512;
constexpr int G1_LDS = G1_CS + 224 * 4;

template <int I> struct ic_t { static constexpr int value = I; };
template <int N, int I = 0, class F> DI void static_for(F&& f) {
  if constexpr (I < N) { f(ic_t<I>{}); static_for<N, I + 1>(f); }
}
template <int N> DI void wait_vm() { asm volatile("s_waitcnt vmcnt(%0)" ::"n"(N) : "memory"); }

constexpr int g1_first(int i) { return i < 0 ? 0 : (i == 0 ? 3 : (i == 1 ? 4 : (i == 2 ? 6 : (i == 3 ? 8 : i + 5)))); }
constexpr int g1_end(int i) { const int e = g1_first(i + 1); return e < G1_NK ? e : G1_NK; }
constexpr int g1_younger(int stage, int last, int P) {
  int n = 0;
  bool seen = false;
  for (int i = -1; i <= last; ++i) {
    const int f = g1_first(i) < G1_NK ? g1_first(i) : G1_NK;
    for (int s2 = f; s2 < g1_end(i); ++s2) { if (seen) n += P; if (s2 == stage) seen = true; }
  }
  return n < 63 ? n : 63;
}

template <bool SIX>
DI void g1_loader(const u16* __restrict__ hT, const char* __restrict__ wp1, const char* __restrict__ wp2, const float* __restrict__ csp,
                  LAS char* sm, const int lw, const int lane, const int fs, const int m0 STAMP_ARG2) {
  constexpr int P = SIX ? 6 : 5;
  const u32 lds0 = (u32)(size_t)sm;
  const srd_t srdW = mk_srd(wp1 + (size_t)fs * 10 * 28672, 10 * 28672);
  const srd_t srdH = mk_srd(hT + m0, (u32)(D_ * L_ - m0) * 2u);
  const u32 voW = lane * 16;
  const int hr = lane >> 4;
  const u32 voH = hr * (L_ * 2) + (((lane & 15) ^ (2 * (4 * (lw & 1) + hr))) * 16);
  const u32 pW = 1024 * lw, h0 = SIX ? lw + 2 : lw - 2;
  u32 lb = lds0, soW = pW, soH = h0 * (4 * L_ * 2);
  auto stage = [&]() {
#pragma unroll
    for (int i = 0; i < (SIX ? 4 : 3); ++i) dma16(lb + pW + 4096 * i, voW, srdW, soW + 4096 * i);
#pragma unroll
    for (int i = 0; i < 2; ++i) dma16(lb + G1_H + 1024 * (h0 + 4 * i), voH, srdH, soH + i * (16 * L_ * 2));
    soW += 14336; soH += 32 * L_ * 2;
    lb = lb == lds0 + (G1_NST - 1) * G1_STAGE ? lds0 : lb + G1_STAGE;
  };
  float cp[10];
  u32 sink = 0;
  constexpr int G1_TAIL = 12;
#pragma unroll
  for (int s2 = g1_first(-1); s2 < g1_end(-1); ++s2) stage();
  wait_vm<g1_younger(0, -1, P)>();
  BAR();
  STAMP_AT(1);
  auto interval = [&](auto ic) {
    constexpr int i = decltype(ic)::value;
#pragma unroll
    for (int s2 = g1_first(i); s2 < g1_end(i); ++s2) stage();
    if (i == G1_NK - 5) {
      const int c = min(64 * lw + lane, 223);
#pragma unroll
      for (int k = 0; k < 10; ++k) cp[k] = csp[(fs * 10 + k) * 224 + c];
      const int jj = blockIdx.x >> 3, fg = (blockIdx.x & 7) >> 1;
      const srd_t srdP = mk_srd(wp2 + (size_t)(fg & 1) * 4 * 276480, 4 * 276480);
      touch(sink, (u32)(jj * 270 + 64 * lw + lane) * 128u, srdP, 0u);
      touch(sink, (u32)(jj * 270 + min(256 + 64 * lw + lane, 269)) * 128u, srdP, 0u);
    }
    if (i + 1 < G1_NK) wait_vm<(g1_younger(i + 1, i, P) + (i >= G1_NK - 5 ? G1_TAIL : 0) < 63 ? g1_younger(i + 1, i, P) + (i >= G1_NK - 5 ? G1_TAIL : 0) : 63)>();
    BAR();
  };
  static_for<G1_NK>(interval);
  touch_done(sink);
  float cs = 0.f;
#pragma unroll
  for (int i = 0; i < 10; ++i) cs += cp[i];
  if (64 * lw + lane < 224) *(LAS float*)(sm + G1_CS + (64 * lw + lane) * 4) = cs;
  __syncthreads();
}

__global__ __launch_bounds__(512, 2) void gemm1(const u16* __restrict__ hT, const char* __restrict__ wp1, const char* __restrict__ wp2,
                                                const float* __restrict__ csp, char* __restrict__ Hp, float* __restrict__ r2 STAMP_ARG) {
  STAMP_DECL();
  STAMP_AT(0);
  extern __shared__ __attribute__((aligned(16))) char smem[];
  LAS char* sm = (LAS char*)smem;
  const u32 lds0 = (u32)(size_t)sm;
  const int tid = threadIdx.x, lane = tid & 63;
  const int w = rfl(tid >> 6);
  const int bid = blockIdx.x, xcd = bid & 7, jj = bid >> 3;
  const int mt = (xcd & 1) * 8 + (jj & 7), fs = (xcd >> 1) * 4 + (jj >> 3);
  const int m0 = mt * 128;

  if (w >= 4) {
    if (w < 6) g1_loader<true>(hT, wp1, wp2, csp, sm, w - 4, lane, fs, m0 STAMP_FWD);
    else g1_loader<false>(hT, wp1, wp2, csp, sm, w - 4, lane, fs, m0 STAMP_FWD);
    STAMP_FLUSH();
    return;
  }

  const int mh = w >> 1, nh = w & 1;
  const int q = lane >> 4, qq = (lane & 15) >> 2, p = lane & 3;
  u32 aW = lds0 + nh * 7168 + lane * 16;
  u32 aH[4];
#pragma unroll
  for (int t = 0; t < 4; ++t) {
    const int tt = 4 * mh + ((t + 2 * nh) & 3);
    const int X = 2 * (4 * (q & 1) + qq);
    aH[t] = lds0 + G1_H + (4 * q + qq) * 256 + (((2 * tt) ^ X) + (p >> 1)) * 16 + 8 * (p & 1);
  }
  f32x4 acc[4][7], accS[2];
#pragma unroll
  for (int t = 0; t < 4; ++t)
#pragma unroll
    for (int j = 0; j < 7; ++j) acc[t][j] = (f32x4){0.f, 0.f, 0.f, 0.f};
  accS[0] = acc[0][0]; accS[1] = acc[0][0];
  u32x4 onesv = {0x3F803F80u, 0x3F803F80u, 0x3F803F80u, 0x3F803F80u};
  asm volatile("" : "+v"(onesv));
  const bf16x8 ones = __builtin_bit_cast(bf16x8, onesv);
  bf16x8 W0[7], W1[7];
  bf16x4 H0[4][2], H1[4][2];
  auto rd = [&](int m, bf16x8 (&W)[7], bf16x4 (&H)[4][2]) {
    if (m < 8) H[m >> 1][m & 1] = trread((LAS char*)(size_t)aH[m >> 1] + (m & 1) * 4096);
    else W[m - 8] = *(LAS bf16x8*)((LAS char*)(size_t)aW + (m - 8) * 1024);
  };
  const int sr = lane & 31;
  u32 sa = lds0 + G1_H + sr * 256 + ((fs ^ (2 * (sr & 7))) * 16) + w * 4, sv, svn, slo, shi;
  float a1 = 0.f, b1 = 0.f, a2 = 0.f, b2 = 0.f;
  auto step = [&](const bf16x8 (&W)[7], const bf16x4 (&H)[4][2], bf16x8 (&Wn)[7], bf16x4 (&Hn)[4][2], u32 adv, const u32& s_cur, u32& s_nxt) {
#pragma unroll
    for (int m = 0; m < 30; ++m) {
      const int j = m >> 2, t = m & 3;
      if (m < 28) acc[t][j] = mfma16(W[j], cat8(H[t][0], H[t][1]), acc[t][j]);
      else accS[m - 28] = mfma16(ones, cat8(H[m - 28][0], H[m - 28][1]), accS[m - 28]);
      if (m < 15) rd(m, Wn, Hn);
      if (m == 15) s_nxt = *(LAS u32*)((LAS char*)(size_t)sa);
      if (m == 16) vadd_s(aW, adv);
      if (m >= 17 && m < 21) vadd_s(aH[m - 17], adv);
      if (m == 21) vadd_s(sa, adv);
      if (m == 22) vshl16(slo, s_cur);
      if (m == 23) vhi16(shi, s_cur);
      if (m == 24) vaddf(a1, slo);
      if (m == 25) vaddf(b1, shi);
      if (m == 26) vsq(a2, slo);
      if (m == 27) vsq(b2, shi);
      PIN();
    }
  };
  auto advance = [&](int ks) {
    return ((ks + 2) % G1_NST == 0) ? (u32)(-(G1_NST - 1) * G1_STAGE) : (u32)G1_STAGE;
  };
  BAR();
  STAMP_AT(2);
  WAIT_L0();
#pragma unroll
  for (int m = 0; m < 15; ++m) rd(m, W0, H0);
  sv = *(LAS u32*)((LAS char*)(size_t)sa);
  aW += G1_STAGE; sa += G1_STAGE;
#pragma unroll
  for (int t = 0; t < 4; ++t) aH[t] += G1_STAGE;
#pragma unroll 1
  for (int ks = 0; ks < G1_NK; ks += 2) {
    WAIT_L0();
    BAR();
    step(W0, H0, W1, H1, advance(ks), sv, svn);
    WAIT_L0();
    BAR();
    step(W1, H1, W0, H0, advance(ks + 1), svn, sv);
  }
  STAMP_AT(6);
  LAS float* st = (LAS float*)(sm + G1_ST);
  if (lane < 16) {
#pragma unroll
    for (int t = 0; t < 2; ++t) st[(4 * mh + ((t + 2 * nh) & 3)) * 16 + lane] = rbf(accS[t][0] * (1.f / D_));
  }
  __syncthreads();
  float mean[4];
#pragma unroll
  for (int t = 0; t < 4; ++t) mean[t] = st[(4 * mh + ((t + 2 * nh) & 3)) * 16 + (lane & 15)];
  const int g0 = ((27 * fs) >> 1) + 7 * nh;
  const srd_t so = mk_srd(Hp, 32 * 54 * 4096);
  f32x4 cv[7];
#pragma unroll
  for (int j = 0; j < 7; ++j) cv[j] = *(LAS f32x4*)(sm + G1_CS + ((7 * nh + j) * 16 + 4 * q) * 4);
#pragma unroll
  for (int pr = 0; pr < 2; ++pr) {
    const int tt = 4 * mh + ((2 * pr + (q & 1) + 2 * nh) & 3);
    const u32 base = (u32)((2 * mt + (tt >> 2)) * 54 * 4096 + (tt & 3) * 1024 + (lane & 15) * 16);
    u32 pk[7][2];
#pragma unroll
    for (int j = 0; j < 7; ++j) {
      const f32x4 a = __builtin_elementwise_fma(cv[j], (f32x4)(-mean[2 * pr]), acc[2 * pr][j]);
      const f32x4 b = __builtin_elementwise_fma(cv[j], (f32x4)(-mean[2 * pr + 1]), acc[2 * pr + 1][j]);
      f32x4 gv, uv;
#pragma unroll
      for (int r = 0; r < 4; ++r) {
        auto s = __builtin_amdgcn_permlane16_swap(__float_as_uint(a[r]), __float_as_uint(b[r]), false, false);
        gv[r] = __uint_as_float(s[0]); uv[r] = __uint_as_float(s[1]);
      }
      const f32x4 pv = gv * uv;
      pk[j][0] = cvtpk(pv[0], pv[1]); pk[j][1] = cvtpk(pv[2], pv[3]);
      if (j & 1) {
        auto s0 = __builtin_amdgcn_permlane32_swap(pk[j - 1][0], pk[j][0], false, false);
        auto s1x = __builtin_amdgcn_permlane32_swap(pk[j - 1][1], pk[j][1], false, false);
        const int gi = g0 + j - 1 + (q >> 1);
        __builtin_amdgcn_raw_buffer_store_b128((u32x4){s0[0], s1x[0], s0[1], s1x[1]}, so, base + (gi >> 2) * 4096 + (gi & 3) * 256, 0, H_AUX);
      }
    }
    const int gi = g0 + 6;
    __builtin_amdgcn_raw_buffer_store_b64((u32x2){pk[6][0], pk[6][1]}, so, base + (gi >> 2) * 4096 + (gi & 3) * 256 + 8 * (q >> 1), 0, H_AUX);
  }
  {
    a1 = dpp_add<0x128>(a1); b1 = dpp_add<0x128>(b1); a2 = dpp_add<0x128>(a2); b2 = dpp_add<0x128>(b2);
    a1 = dpp_add<0x124>(a1); b1 = dpp_add<0x124>(b1); a2 = dpp_add<0x124>(a2); b2 = dpp_add<0x124>(b2);
    a1 = dpp_add<0x122>(a1); b1 = dpp_add<0x122>(b1); a2 = dpp_add<0x122>(a2); b2 = dpp_add<0x122>(b2);
    a1 = dpp_add<0x121>(a1); b1 = dpp_add<0x121>(b1); a2 = dpp_add<0x121>(a2); b2 = dpp_add<0x121>(b2);
    float s1 = (lane & 1) ? b1 : a1, s2 = (lane & 1) ? b2 : a2;
    { auto r = __builtin_amdgcn_permlane16_swap(__float_as_uint(s1), __float_as_uint(s1), false, false); s1 = __uint_as_float(r[0]) + __uint_as_float(r[1]); }
    { auto r = __builtin_amdgcn_permlane16_swap(__float_as_uint(s2), __float_as_uint(s2), false, false); s2 = __uint_as_float(r[0]) + __uint_as_float(r[1]); }
    const float mn = s1 * (1.f / D_);
    const float var = fmaxf(s2 * (1.f / D_) - mn * mn, 0.f);
    const float rs = 1.f / bfhi(ln_pack(mn, var));
    if (lane < 2) r2[m0 + 8 * fs + 2 * w + lane] = rs * rs;
  }
  STAMP_AT(7);
  STAMP_FLUSH();
}

constexpr int G2_H = 10240, G2_STAGE = 18432, G2_NST = 8, G2_NK = 27, G2_RES = G2_NST * G2_STAGE;
constexpr int G2_LDS = G2_RES + 10240;

constexpr int g2_first(int j) { return j < 0 ? 0 : (j == 0 ? 4 : (j == 1 ? 5 : (j == 2 ? 9 : 2 * j + 5))); }
constexpr int g2_end(int j) { const int e = g2_first(j + 1); return e < G2_NK ? e : G2_NK; }
constexpr int G2_RESJ = 6;
constexpr int g2_younger(int tile, int last, int P) {
  int n = 0;
  bool seen = false;
  for (int j = -1; j <= last; ++j) {
    if (seen && j == G2_RESJ) n += 3;
    const int f = g2_first(j) < G2_NK ? g2_first(j) : G2_NK;
    for (int t = f; t < g2_end(j); ++t) { if (seen) n += P; if (t == tile) seen = true; }
  }
  return n < 63 ? n : 63;
}

template <bool ISW>
DI void g2_loader(const char* __restrict__ Hp, const char* __restrict__ wp2, const u16* __restrict__ hT, LAS char* sm, const int lw, const int lane,
                  const int mt, const int nt STAMP_ARG2) {
  constexpr int P = ISW ? 5 : 4;
  const u32 lds0 = (u32)(size_t)sm;
  const srd_t srd = ISW ? mk_srd(wp2 + (size_t)nt * 27 * 10240, 27 * 10240) : mk_srd(Hp + (size_t)mt * 54 * 4096, 54 * 4096);
  const u32 vo = lane * 16;
  const u32 l0 = lds0 + (ISW ? 5120 * lw : G2_H + 4096 * (lw - 2));
  u32 lb = l0, so = ISW ? 5120 * lw : 4096 * (lw - 2);
  auto tile = [&]() {
#pragma unroll
    for (int i = 0; i < P; ++i) dma16(lb + 1024 * i, vo, srd, so + 1024 * i);
    so += ISW ? 10240 : 8192;
    lb = lb == l0 + (G2_NST - 1) * G2_STAGE ? l0 : lb + G2_STAGE;
  };
#pragma unroll
  for (int t = g2_first(-1); t < g2_end(-1); ++t) tile();
  wait_vm<g2_younger(0, -1, P)>();
  BAR();
  STAMP_AT(1);
  auto interval = [&](auto jc) {
    constexpr int j = decltype(jc)::value;
    if (j == G2_RESJ) {
      const srd_t srdR = mk_srd(hT + (size_t)nt * 80 * L_ + mt * 64, (u32)((D_ - nt * 80) * L_ - mt * 64) * 2u);
#pragma unroll
      for (int i = 0; i < 3; ++i) {
        const int pc = min(lw + 4 * i, 9);
        dma16(lds0 + G2_RES + 1024 * pc, (u32)((8 * pc + (lane >> 3)) * (L_ * 2) + (lane & 7) * 16), srdR, 0u);
      }
    }
#pragma unroll
    for (int t = g2_first(j); t < g2_end(j); ++t) tile();
    constexpr int need = 2 * j + 2 < G2_NK ? 2 * j + 2 : G2_NK - 1;
    if (j >= 12) wait_vm<0>(); else wait_vm<g2_younger(need, j, P)>();
    BAR();
  };
  static_for<14>(interval);
  __syncthreads();
}

__global__ __launch_bounds__(512, 2) void gemm2(const char* __restrict__ Hp, const char* __restrict__ wp2, const u16* __restrict__ hT,
                                                const float* __restrict__ r2, u16* __restrict__ out STAMP_ARG) {
  STAMP_DECL();
  STAMP_AT(0);
  extern __shared__ __attribute__((aligned(16))) char smem[];
  LAS char* sm = (LAS char*)smem;
  const u32 lds0 = (u32)(size_t)sm;
  const int tid = threadIdx.x, lane = tid & 63;
  const int w = rfl(tid >> 6);
  const int bid = blockIdx.x, xcd = bid & 7, jj = bid >> 3;
  const int fg = xcd >> 1;
  const int mt = (xcd & 1) * 16 + (fg >> 1) * 8 + (jj & 7), nt = (fg & 1) * 4 + (jj >> 3);
  const int m0 = mt * 64, n0 = nt * 80;

  if (w >= 4) {
    if (w < 6) g2_loader<true>(Hp, wp2, hT, sm, w - 4, lane, mt, nt STAMP_FWD);
    else g2_loader<false>(Hp, wp2, hT, sm, w - 4, lane, mt, nt STAMP_FWD);
    STAMP_FLUSH();
    return;
  }

  const int th = w >> 1, kh = w & 1;
  u32 aW = lds0 + kh * 5120 + lane * 16, aH = lds0 + G2_H + kh * 4096 + th * 2048 + lane * 16;
  f32x4 acc[2][5];
#pragma unroll
  for (int t = 0; t < 2; ++t)
#pragma unroll
    for (int d = 0; d < 5; ++d) acc[t][d] = (f32x4){0.f, 0.f, 0.f, 0.f};
  const float rsc = r2[m0 + 16 * (2 * th + kh) + (lane & 15)];
  bf16x8 W0[5], W1[5], H0[2], H1[2];
  auto rd = [&](int m, bf16x8 (&W)[5], bf16x8 (&H)[2]) {
    if (m < 2) H[m] = *(LAS bf16x8*)((LAS char*)(size_t)aH + m * 1024);
    else W[m - 2] = *(LAS bf16x8*)((LAS char*)(size_t)aW + (m - 2) * 1024);
  };
  int rb = 0;
  auto next = [&]() {
    const u32 adv = rb == G2_NST - 1 ? (u32)(-(G2_NST - 1) * G2_STAGE) : (u32)G2_STAGE;
    rb = rb == G2_NST - 1 ? 0 : rb + 1;
    return adv;
  };
  auto step = [&](const bf16x8 (&W)[5], const bf16x8 (&H)[2], bf16x8 (&Wn)[5], bf16x8 (&Hn)[2]) {
    const u32 adv = next();
#pragma unroll
    for (int m = 0; m < 10; ++m) {
      acc[m & 1][m >> 1] = mfma16(W[m >> 1], H[m & 1], acc[m & 1][m >> 1]);
      if (m < 7) rd(m, Wn, Hn);
      if (m == 7) vadd_s(aW, adv);
      if (m == 8) vadd_s(aH, adv);
      PIN();
    }
  };
  BAR();
  STAMP_AT(2);
  WAIT_L0();
  {
#pragma unroll
    for (int m = 0; m < 7; ++m) rd(m, W0, H0);
    const u32 adv = next(); aW += adv; aH += adv;
  }
#pragma unroll 1
  for (int j = 0; j < 13; ++j) {
    WAIT_L0();
    BAR();
    step(W0, H0, W1, H1);
    WAIT_L0();
    step(W1, H1, W0, H0);
  }
  WAIT_L0();
  BAR();
#pragma unroll
  for (int m = 0; m < 10; ++m) acc[m & 1][m >> 1] = mfma16(W0[m >> 1], H0[m & 1], acc[m & 1][m >> 1]);
  STAMP_AT(6);
  {
    LAS char* ex = sm + w * 5120 + lane * 16;
#pragma unroll
    for (int d = 0; d < 5; ++d) *(LAS f32x4*)(ex + d * 1024) = kh ? acc[0][d] : acc[1][d];
  }
  __syncthreads();
  const int q = lane >> 4, qq = (lane & 15) >> 2, p = lane & 3;
  const int tt = 2 * th + kh;
  u16* orow = out + (size_t)(m0 + 16 * tt + (lane & 15)) * D_ + n0 + 4 * q;
#pragma unroll
  for (int d = 0; d < 5; ++d) {
    const f32x4 o = ((kh ? acc[1][d] : acc[0][d]) + *(LAS f32x4*)(sm + (w ^ 1) * 5120 + lane * 16 + d * 1024)) * rsc;
    const u32x2 hr = __builtin_bit_cast(u32x2, trread(sm + G2_RES + (16 * d + 4 * q + qq) * 128 + 32 * tt + 8 * p));
    const float o0 = bflo(hr[0]) + o[0], o1 = bfhi(hr[0]) + o[1], o2 = bflo(hr[1]) + o[2], o3 = bfhi(hr[1]) + o[3];
    *(u32x2*)(orow + 16 * d) = (u32x2){cvtpk(o0, o1), cvtpk(o2, o3)};
  }
  STAMP_AT(7);
  STAMP_FLUSH();
}

constexpr size_t WS_STAMP = 32u << 20;
constexpr size_t WS_EPOCH = 0, WS_GRAN = 4096, WS_R2 = WS_GRAN + 16384, WS_CS = WS_R2 + 8192, WS_HT = 262144, WS_H = WS_HT + (size_t)D_ * L_ * 2,
                 WS_WP1 = WS_H + (size_t)32 * 54 * 4096, WS_WP2 = WS_WP1 + (size_t)16 * 10 * 28672;

extern "C" void kernel_launch(void* const* d_in, const int* in_sizes, int n_in, void* d_out, int out_size, void* d_ws, size_t ws_size,
                              hipStream_t stream) {
  char* ws = (char*)d_ws;
  const u16* x = (const u16*)d_in[0];
  u16* hT = (u16*)(ws + WS_HT);
  float* csp = (float*)(ws + WS_CS);
  (void)hipFuncSetAttribute((const void*)k12, hipFuncAttributeMaxDynamicSharedMemorySize, K12_LDS);
  (void)hipFuncSetAttribute((const void*)gemm1, hipFuncAttributeMaxDynamicSharedMemorySize, G1_LDS);
  (void)hipFuncSetAttribute((const void*)gemm2, hipFuncAttributeMaxDynamicSharedMemorySize, G2_LDS);
  k12<<<256, 512, K12_LDS, stream>>>(x, (const u16*)d_in[1], (const u16*)d_in[2], (const u16*)d_in[3], (const u16*)d_in[4],
                                     (const u16*)d_in[5], (const u16*)d_in[6], (const u16*)d_in[7], (const u16*)d_in[8],
                                     (const u16*)d_in[10], (const u16*)d_in[11], (const u16*)d_in[12], hT, ws + WS_WP1, ws + WS_WP2, csp,
                                     (u64*)(ws + WS_GRAN), (u32*)(ws + WS_EPOCH) STAMP_PASS(0));
  gemm1<<<256, 512, G1_LDS, stream>>>(hT, ws + WS_WP1, ws + WS_WP2, csp, ws + WS_H, (float*)(ws + WS_R2) STAMP_PASS(1));
  gemm2<<<256, 512, G2_LDS, stream>>>(ws + WS_H, ws + WS_WP2, hT, (const float*)(ws + WS_R2), (u16*)d_out STAMP_PASS(2));
}
```

```cpp
#include <hip/hip_runtime.h>
#include <stdint.h>

typedef unsigned short u16;
typedef unsigned int u32;
typedef unsigned long long u64;
typedef float f32x2 __attribute__((ext_vector_type(2)));
typedef float f32x4 __attribute__((ext_vector_type(4)));
typedef __bf16 bf16x2 __attribute__((ext_vector_type(2)));
typedef __bf16 bf16x4 __attribute__((ext_vector_type(4)));
typedef __bf16 bf16x8 __attribute__((ext_vector_type(8)));
typedef u32 u32x2 __attribute__((ext_vector_type(2)));
typedef u32 u32x4 __attribute__((ext_vector_type(4)));
#define LAS __attribute__((address_space(3)))
#define GAS __attribute__((address_space(1)))
#define DI __device__ __forceinline__
#define RLX_AGENT __ATOMIC_RELAXED, __HIP_MEMORY_SCOPE_AGENT

constexpr int L_ = 2048, D_ = 640, NS_ = 128, F_ = 1728;

DI float bflo(u32 v) { return __uint_as_float(v << 16); }
DI float bfhi(u32 v) { return __uint_as_float(v & 0xffff0000u); }
DI float bf2f(u16 v) { return __uint_as_float((u32)v << 16); }
DI u32 cvtpk(float lo, float hi) {
  f32x2 v = {lo, hi};
  bf16x2 b = __builtin_convertvector(v, bf16x2);
  return __builtin_bit_cast(u32, b);
}
DI float rbf(float x) { return bflo(cvtpk(x, x)); }
DI u32 rfl(u32 x) { return __builtin_amdgcn_readfirstlane(x); }

template <int CTRL>
DI float dpp_add(float v) {
  int t = __builtin_amdgcn_update_dpp(0, __float_as_int(v), CTRL, 0xf, 0xf, true);
  return v + __int_as_float(t);
}
#define CM_MUL(r, a, b) "v_pk_mul_f32 " r ", " a ", " b " op_sel:[0,0] op_sel_hi:[0,1]\n\t"
#define CM_FMA(r, a, b) "v_pk_fma_f32 " r ", " a ", " b ", " r " op_sel:[1,1,0] op_sel_hi:[1,0,1] neg_lo:[0,1,0]\n\t"
DI void cmul2(f32x2& r0, f32x2 a0, f32x2 b0, f32x2& r1, f32x2 a1, f32x2 b1) {
  asm(CM_MUL("%0", "%2", "%3") CM_MUL("%1", "%4", "%5") CM_FMA("%0", "%2", "%3") CM_FMA("%1", "%4", "%5")
      : "=&v"(r0), "=&v"(r1) : "v"(a0), "v"(b0), "v"(a1), "v"(b1));
}
DI void cmul3(f32x2& r0, f32x2 a0, f32x2 b0, f32x2& r1, f32x2 a1, f32x2 b1, f32x2& r2, f32x2 a2, f32x2 b2) {
  asm(CM_MUL("%0", "%3", "%4") CM_MUL("%1", "%5", "%6") CM_MUL("%2", "%7", "%8") CM_FMA("%0", "%3", "%4") CM_FMA("%1", "%5", "%6") CM_FMA("%2", "%7", "%8")
      : "=&v"(r0), "=&v"(r1), "=&v"(r2) : "v"(a0), "v"(b0), "v"(a1), "v"(b1), "v"(a2), "v"(b2));
}
DI void cmul4(f32x2& r0, f32x2 a0, f32x2& r1, f32x2 a1, f32x2& r2, f32x2 a2, f32x2& r3, f32x2 a3, f32x2 b) {
  asm(CM_MUL("%0", "%4", "%8") CM_MUL("%1", "%5", "%8") CM_MUL("%2", "%6", "%8") CM_MUL("%3", "%7", "%8")
      CM_FMA("%0", "%4", "%8") CM_FMA("%1", "%5", "%8") CM_FMA("%2", "%6", "%8") CM_FMA("%3", "%7", "%8")
      : "=&v"(r0), "=&v"(r1), "=&v"(r2), "=&v"(r3) : "v"(a0), "v"(a1), "v"(a2), "v"(a3), "v"(b));
}
DI f32x2 zpow(float ar, float ai, float p) {
  const float er = __builtin_amdgcn_exp2f(ar * p * 1.44269504088896341f);
  const float t = ai * p;
  const float CH = 0.15915494309189535f, CL = 6.4206383e-09f;
  const float hi = t * CH;
  float lo = __builtin_fmaf(t, CH, -hi);
  lo = __builtin_fmaf(t, CL, lo);
  const float fr = __builtin_amdgcn_fractf(hi) + lo;
  f32x2 r = {er * __builtin_amdgcn_cosf(fr), er * __builtin_amdgcn_sinf(fr)};
  return r;
}

DI bf16x8 cat8(bf16x4 a, bf16x4 b) { return __builtin_shufflevector(a, b, 0, 1, 2, 3, 4, 5, 6, 7); }
DI bf16x4 trread(LAS char* p) { return __builtin_amdgcn_ds_read_tr16_b64_v4bf16((LAS bf16x4*)p); }
DI f32x4 mfma16(bf16x8 a, bf16x8 b, f32x4 c) { return __builtin_amdgcn_mfma_f32_16x16x32_bf16(a, b, c, 0, 0, 0); }

DI u32 ln_pack(float mean, float var) {
  const float vb = rbf(var);
  const float t = rbf(vb + 1e-5f);
  const float s = __builtin_sqrtf(t);
  return cvtpk(mean, s);
}

typedef __amdgpu_buffer_rsrc_t srd_t;
DI srd_t mk_srd(const void* p, u32 bytes) { return __builtin_amdgcn_make_buffer_rsrc((void*)p, (short)0, (int)bytes, 0x00020000); }
DI void dma16(u32 lds_addr, u32 voff, srd_t srd, u32 soff) {
  asm volatile("s_mov_b32 m0, %0\n\ts_nop 0\n\tbuffer_load_dwordx4 %1, %2, %3 offen lds" ::"s"(lds_addr), "v"(voff), "s"(srd), "s"(soff) : "memory");
}
DI void touch(u32& sink, u32 voff, srd_t srd, u32 soff) {
  asm volatile("buffer_load_dword %0, %1, %2, %3 offen" : "+v"(sink) : "v"(voff), "s"(srd), "s"(soff) : "memory");
}
DI void touch_done(u32& sink) { asm volatile("s_waitcnt vmcnt(0)" : "+v"(sink)::"memory"); }
template <int I> struct ic_t { static constexpr int value = I; };
template <int N, int I = 0, class F> DI void static_for(F&& f) {
  if constexpr (I < N) { f(ic_t<I>{}); static_for<N, I + 1>(f); }
}
template <int N> DI void wait_vm() { asm volatile("s_waitcnt vmcnt(%0)" ::"n"(N) : "memory"); }
#define WAIT_V(n) asm volatile("s_waitcnt vmcnt(" #n ")" ::: "memory")
#define WAIT_L0() __builtin_amdgcn_s_waitcnt(0xC07F)
#define BAR() __builtin_amdgcn_s_barrier()
#define PIN() __builtin_amdgcn_sched_barrier(0)
DI void vadd_s(u32& a, u32 sadv) { asm volatile("v_add_u32 %0, %1, %0" : "+v"(a) : "s"(sadv)); }
DI void vshl16(u32& d, u32 x) { asm volatile("v_lshlrev_b32 %0, 16, %1" : "=v"(d) : "v"(x)); }
DI void vhi16(u32& d, u32 x) { asm volatile("v_and_b32 %0, 0xffff0000, %1" : "=v"(d) : "v"(x)); }
DI void vaddf(float& a, u32 x) { asm volatile("v_add_f32 %0, %1, %0" : "+v"(a) : "v"(x)); }
DI void vsq(float& a, u32 x) { asm volatile("v_fmac_f32 %0, %1, %1" : "+v"(a) : "v"(x)); }
#define PACK_AUX 16
#define HT_AUX 16
#define H_AUX 16
#define OUT_AUX 0

constexpr int KC_COPY = 4160;
constexpr int KC_CH = 4 * KC_COPY;
constexpr int K12_KC = 0;
constexpr int K12_US = 49920;
constexpr int K12_XS = K12_US + 3 * 4608;
constexpr int KD_CH = 684, KD0 = 0, KD1 = 288, KD2 = 420, KD3 = 552;
constexpr int K12_KD = K12_XS + 3 * 4096;
constexpr int K12_SCR = K12_KD + 6 * KD_CH * 4;
constexpr int K12_FLG = K12_SCR + 6 * 8192;
constexpr int K12_WD = K12_FLG + 32;
constexpr int K12_LDS = K12_WD + 2 * 5760;
constexpr u32 CODE2_BYTES = 8192;
constexpr int NCONV = 220;
constexpr int PK_U = 14352, PK_SLAB = 28928;

extern "C" __global__ __launch_bounds__(512, 2) void s4_k12(const u16* __restrict__ x, const u16* __restrict__ Are, const u16* __restrict__ Aim,
                                              const u16* __restrict__ Bre, const u16* __restrict__ Bim, const u16* __restrict__ Cre,
                                              const u16* __restrict__ Cim, const u16* __restrict__ logdt, const u16* __restrict__ Dsk,
                                              const u16* __restrict__ wg, const u16* __restrict__ wu, const u16* __restrict__ wd,
                                              u16* __restrict__ hT, char* __restrict__ wp1, char* __restrict__ wp2,
                                              float* __restrict__ csp, u64* gran, u32* epoch) {
  extern __shared__ __attribute__((aligned(16))) char smem[];
  LAS char* sm = (LAS char*)smem;
  const u32 lds0 = (u32)(size_t)sm;
  const int tid = threadIdx.x, lane = tid & 63;
  const int wid = rfl(tid >> 6);
  const int bid = blockIdx.x, xcd = bid & 7, slot = bid >> 3;
  const int wq0 = (55 * xcd + 1) >> 1, ncv = ((55 * (xcd + 1) + 1) >> 1) - wq0;
  const int wq = wq0 + slot;
  const bool is_conv = slot < ncv;
  const int cg = wq / 22, cp = wq - 22 * cg;
  const int c0 = is_conv ? 64 * cg + (cp < 20 ? 3 * cp : 59 + 2 * (cp - 20)) : 0;
  u32 sinkc = 0;
  {
    const bool own = slot < 8 && wid == 7;
    const bool later = slot == ncv && wid >= 4;
    if (own || later) {
      u32 lo, hi, lo1, hi1, lo2, hi2;
      asm volatile("s_getpc_b64 s[40:41]\n\ts_add_u32 s40, s40, s4_k12@rel32@lo+4\n\ts_addc_u32 s41, s41, s4_k12@rel32@hi+12\n\t"
                   "s_mov_b32 %0, s40\n\ts_mov_b32 %1, s41\n\t"
                   "s_getpc_b64 s[40:41]\n\ts_add_u32 s40, s40, s4_gemm1@rel32@lo+4\n\ts_addc_u32 s41, s41, s4_gemm1@rel32@hi+12\n\t"
                   "s_mov_b32 %2, s40\n\ts_mov_b32 %3, s41\n\t"
                   "s_getpc_b64 s[40:41]\n\ts_add_u32 s40, s40, s4_gemm2@rel32@lo+4\n\ts_addc_u32 s41, s41, s4_gemm2@rel32@hi+12\n\t"
                   "s_mov_b32 %4, s40\n\ts_mov_b32 %5, s41"
                   : "=s"(lo), "=s"(hi), "=s"(lo1), "=s"(hi1), "=s"(lo2), "=s"(hi2)::"s40", "s41", "scc");
      if (hi != hi1 || hi != hi2 || lo1 - lo > 65536u || lo2 - lo1 > 65536u) {
      } else if (own) {
        const srd_t sc = mk_srd((const char*)(((u64)hi << 32) | lo), lo1 - lo);
        if (lane < 20) touch(sinkc, (u32)(lane * 8 + slot) * 128u, sc, 0u);
      } else {
        const srd_t sc = mk_srd((const char*)(((u64)hi1 << 32) | lo1), (lo2 - lo1) + CODE2_BYTES);
        touch(sinkc, (u32)((wid - 4) * 64 + lane) * 128u, sc, 0u);
        if (wid == 7) {
          const u64 ka = (u64)__builtin_amdgcn_kernarg_segment_ptr() & ~(u64)4095;
          touch(sinkc, (u32)(lane & 31) * 128u, mk_srd((const char*)ka, 4096), 0u);
        }
      }
    }
  }
  const int q = lane >> 4, l16 = lane & 15;
  u32 pa[6], ldt = 0;
  if (is_conv && wid < 6) {
    const int o = (c0 + (wid >> 1)) * NS_ + (wid & 1) * 64 + lane;
    pa[0] = Are[o]; pa[1] = Aim[o]; pa[2] = Bre[o]; pa[3] = Bim[o]; pa[4] = Cre[o]; pa[5] = Cim[o];
    ldt = logdt[c0 + (wid >> 1)];
  }
  const int tok = bid * 8 + 4 * (wid & 1) + q;
  u32x2 xr[10];
  if (wid == 2 || wid == 3) {
    const u32x2* p = (const u32x2*)(x + tok * D_);
#pragma unroll
    for (int i = 0; i < 10; ++i) xr[i] = p[l16 + 16 * i];
  }
  u32 tagv;
  {
    u32 zero = 0;
    asm volatile("" : "+v"(zero));
    tagv = ((const GAS u32*)epoch)[zero];
  }
  const int e0 = min((c0 & ~1), D_ - 4), sh = c0 - e0;
  if (tid < 8) *(LAS u32*)(sm + K12_FLG + tid * 4) = 0u;
  WAIT_L0();
  BAR();
  u32x2 xv[4][4];
  const int rot = tid - 384 + 23 * (wq % 22);
  if (is_conv && wid >= 6) {
#pragma unroll
    for (int g4 = 0; g4 < 4; ++g4)
#pragma unroll
      for (int k = 0; k < 4; ++k) xv[g4][k] = *(const u32x2*)(x + (4 * ((rot + 128 * g4) & 511) + k) * D_ + e0);
  }

  auto ln1 = [&](u32 tag) {
#pragma unroll
    for (int i = 0; i < 10; ++i) asm volatile("" : "+v"(xr[i]));
    const float piv = bflo(__builtin_amdgcn_update_dpp(0u, xr[0][0], 0x150, 0xf, 0xf, true));
    f32x2 s2 = {0.f, 0.f}, q2 = {0.f, 0.f};
    const f32x2 pv2 = {piv, piv};
#pragma unroll
    for (int i = 0; i < 10; ++i)
#pragma unroll
      for (int h = 0; h < 2; ++h) {
        const u32 e = xr[i][h];
        const f32x2 d = (f32x2){bflo(e), bfhi(e)} - pv2;
        s2 += d;
        q2 = __builtin_elementwise_fma(d, d, q2);
      }
    float s = s2.x + s2.y, qs = q2.x + q2.y;
    s = dpp_add<0x128>(s); qs = dpp_add<0x128>(qs);
    s = dpp_add<0x124>(s); qs = dpp_add<0x124>(qs);
    s = dpp_add<0x122>(s); qs = dpp_add<0x122>(qs);
    s = dpp_add<0x121>(s); qs = dpp_add<0x121>(qs);
    const float md = s * (1.f / D_);
    const float var = fmaxf(qs * (1.f / D_) - md * md, 0.f);
    const u32 st = ln_pack(piv + md, var);
    if (l16 == 0) __hip_atomic_store((GAS u64*)(gran + tok), ((u64)tag << 32) | st, RLX_AGENT);
  };
  auto get_tag = [&]() { asm volatile("" : "+v"(tagv)); return rfl(tagv) + 1u; };

  u32 tag = 0;
  if (wid == 2 || wid == 3) { tag = get_tag(); ln1(tag); }
  if (!is_conv) {
    const int sp = 32 * xcd - wq0 + (slot - ncv);
    const int sb = (sp * 160) / 36, nsl = ((sp + 1) * 160) / 36 - sb;
    const int p = lane & 3, qq = l16 >> 2;
    {
      const srd_t sg = mk_srd(wg, D_ * F_ * 2), su = mk_srd(wu, D_ * F_ * 2);
#pragma unroll
      for (int h = 0; h < 2; ++h) {
        const int pr = wid + 8 * h;
        if (pr < 2 * nsl) {
          const int s = sb + (pr >> 1), fs = s / 10, kt = s - 10 * fs, f0 = 8 * ((27 * fs) >> 1);
          const u32 so = (u32)(kt * 64 * F_ + f0) * 2u, lb = lds0 + (pr >> 1) * PK_SLAB + (pr & 1) * PK_U;
#pragma unroll
          for (int i = 0; i < 14; ++i) {
            const int S = 64 * i + lane, rho = S / 14, sgm = S - 14 * rho;
            dma16(lb + 1024 * i, (u32)(rho * (F_ * 2) + sgm * 16), (pr & 1) ? su : sg, so);
          }
        }
      }
    }
    touch_done(sinkc);
    BAR();
    {
      const srd_t sp = mk_srd(wp1, 16 * 10 * 28672);
      const int rd = (p & 1) * PK_U + (4 * q + qq) * 224 + 8 * (p >> 1);
      const bf16x2 ones = {(__bf16)1.0f, (__bf16)1.0f};
#pragma unroll 1
      for (int job = wid; job < 14 * nsl; job += 8) {
        const int sl = job / 14, ct = job - 14 * sl;
        const int s = sb + sl, fs = s / 10, kt = s - 10 * fs;
        LAS char* b = sm + sl * PK_SLAB + rd + ct * 16;
        float cs = 0.f;
#pragma unroll
        for (int ks = 0; ks < 2; ++ks) {
          const bf16x8 wf = cat8(trread(b + ks * 7168), trread(b + ks * 7168 + 3584));
          const u32x4 wv = __builtin_bit_cast(u32x4, wf);
#pragma unroll
          for (int i = 0; i < 4; ++i) { const u32 e = wv[i]; cs = __builtin_amdgcn_fdot2_f32_bf16(__builtin_bit_cast(bf16x2, e), ones, cs, false); }
          __builtin_amdgcn_raw_buffer_store_b128(wv, sp, (u32)((((fs * 10 + kt) * 2 + ks) * 14 + ct) * 1024 + lane * 16), 0, PACK_AUX);
        }
        { auto r = __builtin_amdgcn_permlane16_swap(__float_as_uint(cs), __float_as_uint(cs), false, false); cs = __uint_as_float(r[0]) + __uint_as_float(r[1]); }
        { auto r = __builtin_amdgcn_permlane32_swap(__float_as_uint(cs), __float_as_uint(cs), false, false); cs = __uint_as_float(r[0]) + __uint_as_float(r[1]); }
        if (lane < 16) csp[(fs * 10 + kt) * 224 + ct * 16 + lane] = cs;
      }
    }
    return;
  }

  u64 g[4];
  auto look = [&]() {
#pragma unroll
    for (int k = 0; k < 4; ++k) g[k] = __hip_atomic_load((GAS u64*)(gran + 4 * tid + k), RLX_AGENT);
  };
  if (wid >= 6) {
    look();
#pragma unroll
    for (int g4 = 0; g4 < 4; ++g4) {
      u32 lo[4], hi[4];
#pragma unroll
      for (int k = 0; k < 4; ++k) {
        lo[k] = sh ? __builtin_amdgcn_alignbit(xv[g4][k][1], xv[g4][k][0], 16) : xv[g4][k][0];
        hi[k] = sh ? (xv[g4][k][1] >> 16) : xv[g4][k][1];
      }
      LAS char* dst = sm + K12_XS + ((rot + 128 * g4) & 511) * 8;
      *(LAS u32x2*)(dst) = (u32x2){__builtin_amdgcn_perm(lo[1], lo[0], 0x05040100u), __builtin_amdgcn_perm(lo[3], lo[2], 0x05040100u)};
      *(LAS u32x2*)(dst + 4096) = (u32x2){__builtin_amdgcn_perm(lo[1], lo[0], 0x07060302u), __builtin_amdgcn_perm(lo[3], lo[2], 0x07060302u)};
      *(LAS u32x2*)(dst + 8192) = (u32x2){__builtin_amdgcn_perm(hi[1], hi[0], 0x05040100u), __builtin_amdgcn_perm(hi[3], hi[2], 0x05040100u)};
    }
    asm volatile("" : "+v"(sinkc)::"memory");
    tag = get_tag();
  } else {
    {
      asm volatile("" : "+v"(ldt), "+v"(pa[0]), "+v"(pa[1]), "+v"(pa[2]), "+v"(pa[3]), "+v"(pa[4]), "+v"(pa[5]));
      const float dt = rbf(expf(bflo(ldt)));
      const float ar = dt * bflo(pa[0]), ai = dt * bflo(pa[1]);
      const float br = dt * bflo(pa[2]), bi = dt * bflo(pa[3]);
      const float cr = bflo(pa[4]), ci = bflo(pa[5]);
      const f32x2 c = {cr * br - ci * bi, cr * bi + ci * br};
      const f32x2 z16 = zpow(ar, ai, 16.f), y1 = zpow(ar, -ai, 1.f), y256 = zpow(ar, -ai, 256.f);
      LAS char* s0 = sm + K12_SCR + wid * 8192;
      LAS char* s1 = s0 + 4096;
      const int sw = ((lane >> 2) & 1) * 16;
      auto put = [&](LAS char* base, const f32x2 (&v)[16], bool both) {
        u32x4 a, b, d, e;
#pragma unroll
        for (int i = 0; i < 4; ++i) {
          a[i] = cvtpk(v[2 * i].x, v[2 * i + 1].x); d[i] = cvtpk(v[2 * i].y, v[2 * i + 1].y);
          if (both || i == 0) { b[i] = cvtpk(v[8 + 2 * i].x, v[9 + 2 * i].x); e[i] = cvtpk(v[8 + 2 * i].y, v[9 + 2 * i].y); }
          else { b[i] = 0u; e[i] = 0u; }
        }
        LAS char* w = base + lane * 32;
        *(LAS u32x4*)(w + sw) = a; *(LAS u32x4*)(w + (sw ^ 16)) = b;
        *(LAS u32x4*)(w + 2048 + sw) = d; *(LAS u32x4*)(w + 2048 + (sw ^ 16)) = e;
      };
      f32x2 Lp[16], P[16], zb[9];
      Lp[0] = c; P[0] = (f32x2){1.f, 0.f}; P[1] = y1; zb[1] = y256;
      cmul2(Lp[1], Lp[0], z16, zb[2], zb[1], y256);
#pragma unroll
      for (int i = 2; i < 8; ++i) cmul3(Lp[i], Lp[i - 1], z16, P[i], P[i - 1], y1, zb[i + 1], zb[i], y256);
#pragma unroll
      for (int i = 8; i < 16; ++i) cmul2(Lp[i], Lp[i - 1], z16, P[i], P[i - 1], y1);
      put(s0, Lp, true);
      put(s1, P, true);
      const int rdo = (4 * q + (l16 >> 2)) * 32 + ((((lane & 3) >> 1) ^ (q & 1)) * 16) + (lane & 1) * 8;
      bf16x8 Lf[4];
#pragma unroll
      for (int s = 0; s < 4; ++s) Lf[s] = cat8(trread(s0 + rdo + s * 1024), trread(s0 + rdo + s * 1024 + 512));
      auto mm = [&]() {
        f32x4 acc = {0.f, 0.f, 0.f, 0.f};
#pragma unroll
        for (int s = 0; s < 4; ++s) {
          const bf16x8 hf = cat8(trread(s1 + rdo + s * 1024), trread(s1 + rdo + s * 1024 + 512));
          acc = mfma16(Lf[s], hf, acc);
        }
        return acc;
      };
      LAS float* kd = (LAS float*)(sm + K12_KD) + ((wid & 1) * 3 + (wid >> 1)) * KD_CH;
      if (lane < 16) kd[KD0 + lane] = 0.f;
      const f32x4 dA = mm();
      f32x2 v[16];
      v[0] = zb[1]; v[8] = zb[2]; v[12] = zb[3];
      cmul4(v[1], P[2], v[2], P[4], v[3], P[6], v[4], P[8], y256);
      cmul3(v[5], P[10], y256, v[6], P[12], y256, v[7], P[14], y256);
      cmul3(v[9], P[4], zb[2], v[10], P[8], zb[2], v[11], P[12], zb[2]);
      cmul3(v[13], P[4], zb[3], v[14], P[8], zb[3], v[15], P[12], zb[3]);
      put(s1, v, true);
      const f32x4 dB = mm();
#pragma unroll
      for (int i = 0; i < 4; ++i) v[2 * i] = zb[4 + i];
      cmul4(v[1], zb[4], v[3], zb[5], v[5], zb[6], v[7], zb[7], P[8]);
      v[8] = zb[8]; v[9] = (f32x2){0.f, 0.f};
      put(s1, v, false);
      const f32x4 dC = mm();
      const int h = l16;
      const int iB = h < 8 ? KD1 + h + 32 * q : KD2 + 64 * ((h - 8) >> 2) + (h & 3) + 16 * q, sB = h < 8 ? 8 : 4;
#pragma unroll
      for (int r = 0; r < 4; ++r) {
        kd[KD0 + 16 + 16 * (4 * q + r) + h] = dA[r];
        kd[iB + sB * r] = dB[r];
        if (h < 8) kd[KD3 + 32 * (h >> 1) + 2 * (4 * q + r) + (h & 1)] = dC[r];
      }
      if (q == 0) {
        if (h < 3) kd[KD0 + 16 + 256 + h + (h > 0)] = dB[0];
        if (h == 0) { kd[KD0 + 16 + 257] = dB[0]; kd[KD2 + 128] = dC[0]; kd[KD2 + 129] = dC[0]; }
        if (h == 8) { kd[KD1 + 128] = dB[0]; kd[KD1 + 129] = dB[0]; kd[KD3 + 128] = dC[0]; }
        if (h == 9) kd[KD1 + 130] = dB[0];
      }
    }
    if (wid != 2 && wid != 3) tag = get_tag();
    {
      LAS u32* fl = (LAS u32*)(sm + K12_FLG);
      WAIT_L0();
      if (lane == 0) __hip_atomic_store(fl + wid, 1u, __ATOMIC_RELAXED, __HIP_MEMORY_SCOPE_WORKGROUP);
      for (;;) {
        const u32 v = __hip_atomic_load(fl + min(lane, 5), __ATOMIC_RELAXED, __HIP_MEMORY_SCOPE_WORKGROUP);
        if (__all(v != 0u)) break;
        __builtin_amdgcn_s_sleep(1);
      }
      asm volatile("" ::: "memory");
    }
    look();
    constexpr int HB = 3 * KD_CH;
    float s[3][11];
    int ch[3], M[3];
    bool act[3];
    auto win0 = [&](int T, int i) {
      act[i] = T < 102;
      ch[i] = min(T / 34, 2);
      const int t = min(T - ch[i] * 34, 33);
      M[i] = -8 + 8 * t;
      LAS float* kd = (LAS float*)(sm + K12_KD) + ch[i] * KD_CH + KD0 + 8 * t;
      const f32x4 u = *(LAS f32x4*)kd + *(LAS f32x4*)(kd + HB), v = *(LAS f32x4*)(kd + 4) + *(LAS f32x4*)(kd + HB + 4),
                  w = *(LAS f32x4*)(kd + 8) + *(LAS f32x4*)(kd + HB + 8);
      s[i][0] = w[3]; s[i][1] = w[2]; s[i][2] = w[1]; s[i][3] = w[0]; s[i][4] = v[3]; s[i][5] = v[2]; s[i][6] = v[1]; s[i][7] = v[0];
      s[i][8] = u[3]; s[i][9] = u[2]; s[i][10] = u[1];
    };
    {
      ch[0] = wid >> 1; act[0] = true;
      const int t = (wid & 1) * 64 + lane;
      M[0] = 1032 + 8 * t;
      LAS float* kd = (LAS float*)(sm + K12_KD) + ch[0] * KD_CH + KD3 + t;
      const float b = kd[0] + kd[HB], a = kd[1] + kd[HB + 1];
#pragma unroll
      for (int i = 0; i < 7; ++i) s[0][i] = a;
      s[0][7] = (t & 1) ? a : b;
      s[0][8] = s[0][9] = s[0][10] = b;
    }
    if (wid < 3) {
      ch[1] = wid; act[1] = true;
      M[1] = 520 + 8 * lane;
      LAS float* kd = (LAS float*)(sm + K12_KD) + wid * KD_CH + KD2 + 2 * lane;
      const f32x2 u = *(LAS f32x2*)kd + *(LAS f32x2*)(kd + HB), v = *(LAS f32x2*)(kd + 2) + *(LAS f32x2*)(kd + HB + 2);
      s[1][0] = v.y; s[1][1] = s[1][2] = s[1][3] = s[1][4] = s[1][5] = v.x; s[1][6] = s[1][7] = s[1][8] = u.y; s[1][9] = s[1][10] = u.x;
    } else if (wid < 5) {
      const int T = (wid - 3) * 64 + lane;
      act[1] = T < 96;
      ch[1] = min(T >> 5, 2);
      const int t = T & 31;
      M[1] = 264 + 8 * t;
      LAS float* kd = (LAS float*)(sm + K12_KD) + ch[1] * KD_CH + KD1 + 4 * t;
      const f32x4 u = *(LAS f32x4*)kd + *(LAS f32x4*)(kd + HB), v = *(LAS f32x4*)(kd + 4) + *(LAS f32x4*)(kd + HB + 4);
      s[1][0] = v[2]; s[1][1] = v[1]; s[1][2] = s[1][3] = s[1][4] = v[0]; s[1][5] = u[3]; s[1][6] = s[1][7] = s[1][8] = u[2]; s[1][9] = u[1]; s[1][10] = u[0];
    } else {
      win0(lane, 1);
    }
    act[2] = false;
    if (wid == 0) win0(64 + lane, 2);
#pragma unroll
    for (int i = 0; i < 3; ++i) {
      if (i == 2 && wid != 0) break;
      if (act[i]) {
        u32 E[5], O[5];
#pragma unroll
        for (int k = 0; k < 5; ++k) { E[k] = cvtpk(s[i][2 * k], s[i][2 * k + 1]); O[k] = cvtpk(s[i][2 * k + 1], s[i][2 * k + 2]); }
        LAS char* dst = sm + K12_KC + ch[i] * KC_CH + 2 * (2048 - M[i]);
        *(LAS u32x4*)(dst + 3 * KC_COPY) = (u32x4){E[0], E[1], E[2], E[3]};
        *(LAS u32x4*)(dst + 2 * KC_COPY) = (u32x4){O[0], O[1], O[2], O[3]};
        *(LAS u32x4*)(dst + 1 * KC_COPY) = (u32x4){E[1], E[2], E[3], E[4]};
        *(LAS u32x4*)(dst + 0 * KC_COPY) = (u32x4){O[1], O[2], O[3], O[4]};
      }
    }
  }
  f32x4 accA[8], accB[8];
#pragma unroll
  for (int t = 0; t < 8; ++t) { accA[t] = (f32x4){0.f, 0.f, 0.f, 0.f}; accB[t] = accA[t]; asm volatile("" : "+v"(accA[t]), "+v"(accB[t])); }
  float dsk[3];
#pragma unroll
  for (int k = 0; k < 3; ++k) dsk[k] = bf2f(Dsk[c0 + k]);
  if (tid < 384) *(LAS u32*)(sm + K12_US + (tid >> 7) * 4608 + (tid & 127) * 4) = 0u;
  __syncthreads();
  float m[4], r[4];
  {
    u32 spins = 0;
    for (;;) {
      bool ok = true;
#pragma unroll
      for (int k = 0; k < 4; ++k) ok &= (u32)(g[k] >> 32) == tag;
      if (__all(ok)) break;
      if (++spins > (1u << 14)) {
#pragma unroll 1
        for (int k = 0; k < 4; ++k) {
          if ((u32)(g[k] >> 32) != tag) {
            const u16* p = x + (4 * tid + k) * D_;
            float s = 0.f;
            for (int i = 0; i < D_; ++i) s += bf2f(p[i]);
            const float mean = s * (1.f / D_);
            float qs = 0.f;
            for (int i = 0; i < D_; ++i) { const float d = bf2f(p[i]) - mean; qs = __builtin_fmaf(d, d, qs); }
            const u64 v = ((u64)tag << 32) | ln_pack(mean, qs * (1.f / D_));
            if (k == 0) g[0] = v; else if (k == 1) g[1] = v; else if (k == 2) g[2] = v; else g[3] = v;
          }
        }
        break;
      }
      __builtin_amdgcn_s_sleep(2);
      look();
    }
#pragma unroll
    for (int k = 0; k < 4; ++k) { m[k] = bflo((u32)g[k]); r[k] = __builtin_amdgcn_rcpf(bfhi((u32)g[k])); }
  }
  {
    const f32x2 m01 = {m[0], m[1]}, m23 = {m[2], m[3]}, r01 = {r[0], r[1]}, r23 = {r[2], r[3]};
#pragma unroll
    for (int c = 0; c < 3; ++c) {
      const u32x2 px = *(LAS u32x2*)(sm + K12_XS + c * 4096 + tid * 8);
      const u32 e0v = px[0], e1v = px[1];
      const f32x2 a = ((f32x2){bflo(e0v), bfhi(e0v)} - m01) * r01, b = ((f32x2){bflo(e1v), bfhi(e1v)} - m23) * r23;
      *(LAS u32x2*)(sm + K12_US + c * 4608 + 512 + tid * 8) = (u32x2){cvtpk(a.x, a.y), cvtpk(b.x, b.y)};
    }
  }
  bf16x4 A0[8][2], A1[8][2];
  bf16x8 B0[8], B1[8];
  const int kap = l16 & 3;
  const int kb0 = K12_KC + kap * KC_COPY + 2 * (2048 - 16 - l16 + 8 * q + kap) - 512 * 7;
  const int ub0 = K12_US + 480 + 32 * l16 + 16 * q;
  auto adr = [&](int un, LAS char*& kb, LAS char*& ub) {
    const int U = 6 * (wid & 3) + min(un, 5), ch = U >> 3, rs = (U & 7) * 2;
    kb = sm + kb0 + ch * KC_CH - 32 * rs;
    ub = sm + ub0 + ch * 4608 - 32 * rs;
  };
  auto rd = [&](int mm, LAS char* kb, LAS char* ub, bf16x4 (&A)[8][2], bf16x8 (&B)[8]) {
    const int i = mm / 3, k = mm - 3 * i;
    if (k == 0) B[i] = *(LAS bf16x8*)(ub + 512 * i);
    else A[i][k - 1] = *(LAS bf16x4*)(kb + 512 * (7 - i) + 8 * (k - 1));
  };
  LAS char *kb1, *ub1;
  adr(0, kb1, ub1);
  if (wid < 4) {
#pragma unroll
    for (int mm = 0; mm < 24; ++mm) if (mm % 3) { rd(mm, kb1, ub1, A0, B0); PIN(); }
  }
  __syncthreads();
  if (bid == 0 && tid == 0) __hip_atomic_store((GAS u32*)epoch, tag, RLX_AGENT);
  if (wid >= 6 && wq < 216) {
    const int ks = wid - 6, nt = wq / 27, kt = wq - 27 * nt;
    const srd_t sd = mk_srd(wd, F_ * D_ * 2);
    const u32 so = (u32)((kt * 64 + ks * 32) * D_ + nt * 80) * 2u, lb = lds0 + K12_WD + ks * 5760;
    const int rho = lane / 10, sgm = lane - 10 * rho;
    if (lane < 60) {
#pragma unroll
      for (int i = 0; i < 6; ++i) {
        if (i < 5 || rho < 2) dma16(lb + 960 * i, (u32)((6 * i + rho) * (D_ * 2) + sgm * 16), sd, so);
      }
    }
    WAIT_V(0);
    const srd_t sp = mk_srd(wp2, 8 * 27 * 10240);
    const int p = lane & 3, qq = l16 >> 2;
    LAS char* b = sm + K12_WD + ks * 5760 + (8 * q + qq) * 160 + 8 * p;
#pragma unroll
    for (int dt = 0; dt < 5; ++dt) {
      const bf16x8 wf = cat8(trread(b + dt * 32), trread(b + 4 * 160 + dt * 32));
      __builtin_amdgcn_raw_buffer_store_b128(__builtin_bit_cast(u32x4, wf), sp, (u32)(((wq * 2 + ks) * 5 + dt) * 1024 + lane * 16), 0, PACK_AUX);
    }
  }
  if (wid < 4) {
#pragma unroll
    for (int mm = 0; mm < 24; mm += 3) rd(mm, kb1, ub1, A0, B0);
    const int n1 = wid == 1 ? 1 : (wid == 2 ? 2 : 3);
    u32 ka, ua;
    {
      LAS char *kb, *ub;
      adr(1, kb, ub);
      ka = (u32)(size_t)kb; ua = (u32)(size_t)ub;
    }
    auto unit = [&](f32x4 (&acc)[8], const bf16x4 (&A)[8][2], const bf16x8 (&B)[8], bf16x4 (&An)[8][2], bf16x8 (&Bn)[8], int un) {
      const int U = 6 * wid + un + 1;
      const bool last = un + 2 > 5, sw = (U & 7) == 7;
      const u32 dk = last ? 0u : (sw ? (u32)(KC_CH + 448) : (u32)-64), du = last ? 0u : (sw ? (u32)(4608 + 448) : (u32)-64);
      int mm = 0;
#pragma unroll
      for (int i = 0; i < 8; ++i)
#pragma unroll
        for (int t = 7; t >= i; --t) {
          acc[t] = mfma16(cat8(A[i][0], A[i][1]), B[t - i], acc[t]);
          if (mm < 24) rd(mm, (LAS char*)(size_t)ka, (LAS char*)(size_t)ua, An, Bn);
          if (mm == 24) vadd_s(ka, dk);
          if (mm == 25) vadd_s(ua, du);
          ++mm;
          PIN();
        }
    };
    auto pair = [&](f32x4 (&acc)[8], int it) {
      WAIT_L0();
      unit(acc, A0, B0, A1, B1, 2 * it);
      WAIT_L0();
      unit(acc, A1, B1, A0, B0, 2 * it + 1);
    };
#pragma unroll 1
    for (int it = 0; it < n1; ++it) pair(accA, it);
#pragma unroll 1
    for (int it = n1; it < 3; ++it) pair(accB, it);
    LAS char* pp = sm + K12_SCR + lane * 16;
    const int sA = wid == 0 ? 0 : 2 * wid - 1;
#pragma unroll
    for (int t = 0; t < 8; ++t) *(LAS f32x4*)(pp + sA * 8192 + t * 1024) = accA[t];
    if (wid == 1 || wid == 2) {
#pragma unroll
      for (int t = 0; t < 8; ++t) *(LAS f32x4*)(pp + 2 * wid * 8192 + t * 1024) = accB[t];
    }
  }
  __syncthreads();
  {
    const srd_t so = mk_srd(hT, D_ * L_ * 2);
#pragma unroll
    for (int i = 0; i < 3; ++i) {
      const int pc = wid * 3 + i, ch = pc >> 3, t = pc & 7;
      const float dk = ch == 0 ? dsk[0] : (ch == 1 ? dsk[1] : dsk[2]);
      LAS char* pp = sm + K12_SCR + 2 * ch * 8192 + t * 1024 + lane * 16;
      const f32x4 y = *(LAS f32x4*)pp + *(LAS f32x4*)(pp + 8192);
      const int l0 = 256 * t + 16 * l16 + 4 * q;
      const u32x2 xx = *(LAS u32x2*)(sm + K12_XS + ch * 4096 + 2 * l0);
      const u32x2 uu = *(LAS u32x2*)(sm + K12_US + ch * 4608 + 512 + 2 * l0);
      const float h0 = bflo(xx[0]) + __builtin_fmaf(bflo(uu[0]), dk, y[0]);
      const float h1 = bfhi(xx[0]) + __builtin_fmaf(bfhi(uu[0]), dk, y[1]);
      const float h2 = bflo(xx[1]) + __builtin_fmaf(bflo(uu[1]), dk, y[2]);
      const float h3 = bfhi(xx[1]) + __builtin_fmaf(bfhi(uu[1]), dk, y[3]);
      if (!(cp >= 20 && ch == 0)) __builtin_amdgcn_raw_buffer_store_b64((u32x2){cvtpk(h0, h1), cvtpk(h2, h3)}, so, (u32)((c0 + ch) * L_ + l0) * 2u, 0, HT_AUX);
    }
  }
}

#define G1_RING 6
constexpr int G1_H = 14336, G1_STAGE = 22528, G1_NST = G1_RING, G1_NK = 20;
constexpr int G1_ST = G1_NST * G1_STAGE, G1_CS = G1_ST + 512;
constexpr int G1_LDS = G1_CS + 224 * 4;
static_assert(G1_LDS <= 163840, "LDS");
constexpr int G1_SLOTS = 30;

constexpr int g1_end(int i) {
  int e = 0;
  for (int k = -1; k <= i; ++k) {
    const int per = k < 0 ? 3 : (k == 0 ? 1 : 2), lim = k < 0 ? G1_NST : k + G1_NST;
    e = e + per < lim ? e + per : lim;
    e = e < G1_NK ? e : G1_NK;
  }
  return e;
}
constexpr int g1_first(int i) { return i < 0 ? 0 : g1_end(i - 1); }
constexpr int g1_younger(int stage, int last, int P) {
  int n = 0;
  bool seen = false;
  for (int i = -1; i <= last; ++i) {
    for (int s2 = g1_first(i); s2 < g1_end(i); ++s2) { if (seen) n += P; if (s2 == stage) seen = true; }
  }
  return n < 63 ? n : 63;
}

template <bool SIX>
DI void g1_loader(const u16* __restrict__ hT, const char* __restrict__ wp1, const char* __restrict__ wp2, const float* __restrict__ csp,
                  LAS char* sm, const int lw, const int lane, const int fs, const int m0) {
  constexpr int P = SIX ? 6 : 5;
  const u32 lds0 = (u32)(size_t)sm;
  const srd_t srdW = mk_srd(wp1 + (size_t)fs * 10 * 28672, 10 * 28672);
  const srd_t srdH = mk_srd(hT + m0, (u32)(D_ * L_ - m0) * 2u);
  const u32 voW = lane * 16;
  const int hr = lane >> 4;
  const u32 voH = hr * (L_ * 2) + (((lane & 15) ^ (2 * (4 * (lw & 1) + hr))) * 16);
  const u32 pW = 1024 * lw, h0 = SIX ? lw + 2 : lw - 2;
  u32 lb = lds0, soW = pW, soH = h0 * (4 * L_ * 2);
  auto stage = [&]() {
#pragma unroll
    for (int i = 0; i < (SIX ? 4 : 3); ++i) dma16(lb + pW + 4096 * i, voW, srdW, soW + 4096 * i);
#pragma unroll
    for (int i = 0; i < 2; ++i) dma16(lb + G1_H + 1024 * (h0 + 4 * i), voH, srdH, soH + i * (16 * L_ * 2));
    soW += 14336; soH += 32 * L_ * 2;
    lb = lb == lds0 + (G1_NST - 1) * G1_STAGE ? lds0 : lb + G1_STAGE;
  };
  float cp[10];
  u32 sink = 0;
  constexpr int G1_CPL = 10;
  constexpr int G1_TAIL = G1_CPL + 2;
#pragma unroll
  for (int s2 = g1_first(-1); s2 < g1_end(-1); ++s2) stage();
  wait_vm<g1_younger(0, -1, P)>();
  BAR();
  auto interval = [&](auto ic) {
    constexpr int i = decltype(ic)::value;
#pragma unroll
    for (int s2 = g1_first(i); s2 < g1_end(i); ++s2) stage();
    if (i == G1_NK - 5) {
      const int c = min(64 * lw + lane, 223);
#pragma unroll
      for (int k = 0; k < 10; ++k) cp[k] = csp[(fs * 10 + k) * 224 + c];
      const int jj = blockIdx.x >> 3, fg = (blockIdx.x & 7) >> 1;
      const srd_t srdP = mk_srd(wp2 + (size_t)(fg & 1) * 4 * 276480, 4 * 276480);
      touch(sink, (u32)(jj * 270 + 64 * lw + lane) * 128u, srdP, 0u);
      touch(sink, (u32)(jj * 270 + min(256 + 64 * lw + lane, 269)) * 128u, srdP, 0u);
    }
    if (i + 1 < G1_NK) wait_vm<(g1_younger(i + 1, i, P) + (i >= G1_NK - 5 ? G1_TAIL : 0) < 63 ? g1_younger(i + 1, i, P) + (i >= G1_NK - 5 ? G1_TAIL : 0) : 63)>();
    BAR();
  };
  static_for<G1_NK>(interval);
  touch_done(sink);
  float cs = 0.f;
#pragma unroll
  for (int i = 0; i < 10; ++i) cs += cp[i];
  if (64 * lw + lane < 224) *(LAS float*)(sm + G1_CS + (64 * lw + lane) * 4) = cs;
  __syncthreads();
}

extern "C" __global__ __launch_bounds__(512, 2) void s4_gemm1(const u16* __restrict__ hT, const char* __restrict__ wp1, const char* __restrict__ wp2,
                                                const float* __restrict__ csp, char* __restrict__ Hp, float* __restrict__ r2) {
  extern __shared__ __attribute__((aligned(16))) char smem[];
  LAS char* sm = (LAS char*)smem;
  const u32 lds0 = (u32)(size_t)sm;
  const int tid = threadIdx.x, lane = tid & 63;
  const int w = rfl(tid >> 6);
  const int bid = blockIdx.x, xcd = bid & 7, jj = bid >> 3;
  const int mt = (xcd & 1) * 8 + (jj & 7), fs = (xcd >> 1) * 4 + (jj >> 3);
  const int m0 = mt * 128;

  if (w >= 4) {
    if (w < 6) g1_loader<true>(hT, wp1, wp2, csp, sm, w - 4, lane, fs, m0);
    else g1_loader<false>(hT, wp1, wp2, csp, sm, w - 4, lane, fs, m0);
    return;
  }

  const int mh = w >> 1, nh = w & 1;
  const int q = lane >> 4, qq = (lane & 15) >> 2, p = lane & 3;
  u32 aW = lds0 + nh * 7168 + lane * 16;
  u32 aH[4];
#pragma unroll
  for (int t = 0; t < 4; ++t) {
    const int tt = 4 * mh + ((t + 2 * nh) & 3);
    const int X = 2 * (4 * (q & 1) + qq);
    aH[t] = lds0 + G1_H + (4 * q + qq) * 256 + (((2 * tt) ^ X) + (p >> 1)) * 16 + 8 * (p & 1);
  }
  f32x4 acc[4][7], accS[2];
#pragma unroll
  for (int t = 0; t < 4; ++t)
#pragma unroll
    for (int j = 0; j < 7; ++j) acc[t][j] = (f32x4){0.f, 0.f, 0.f, 0.f};
  accS[0] = acc[0][0]; accS[1] = acc[0][0];
  u32x4 onesv = {0x3F803F80u, 0x3F803F80u, 0x3F803F80u, 0x3F803F80u};
  asm volatile("" : "+v"(onesv));
  const bf16x8 ones = __builtin_bit_cast(bf16x8, onesv);
  bf16x8 W0[7], W1[7];
  bf16x4 H0[4][2], H1[4][2];
  auto rd = [&](int m, bf16x8 (&W)[7], bf16x4 (&H)[4][2]) {
    if (m < 8) H[m >> 1][m & 1] = trread((LAS char*)(size_t)aH[m >> 1] + (m & 1) * 4096);
    else W[m - 8] = *(LAS bf16x8*)((LAS char*)(size_t)aW + (m - 8) * 1024);
  };
  const int sr = lane & 31;
  u32 sa = lds0 + G1_H + sr * 256 + ((fs ^ (2 * (sr & 7))) * 16) + w * 4, sv, svn, slo, shi;
  float a1 = 0.f, b1 = 0.f, a2 = 0.f, b2 = 0.f;
  auto step = [&](const bf16x8 (&W)[7], const bf16x4 (&H)[4][2], bf16x8 (&Wn)[7], bf16x4 (&Hn)[4][2], u32 adv, const u32& s_cur, u32& s_nxt) {
#pragma unroll
    for (int m = 0; m < G1_SLOTS; ++m) {
      const int j = m >> 2, t = m & 3;
      if (m < 28) acc[t][j] = mfma16(W[j], cat8(H[t][0], H[t][1]), acc[t][j]);
      else accS[m - 28] = mfma16(ones, cat8(H[m - 28][0], H[m - 28][1]), accS[m - 28]);
      if (m < 15) rd(m, Wn, Hn);
      if (m == 15) s_nxt = *(LAS u32*)((LAS char*)(size_t)sa);
      if (m == 16) vadd_s(aW, adv);
      if (m >= 17 && m < 21) vadd_s(aH[m - 17], adv);
      if (m == 21) vadd_s(sa, adv);
      if (m == 22) vshl16(slo, s_cur);
      if (m == 23) vhi16(shi, s_cur);
      if (m == 24) vaddf(a1, slo);
      if (m == 25) vaddf(b1, shi);
      if (m == 26) vsq(a2, slo);
      if (m == 27) vsq(b2, shi);
      PIN();
    }
  };
  auto advance = [&](int ks) {
    return ((ks + 2) % G1_NST == 0) ? (u32)(-(G1_NST - 1) * G1_STAGE) : (u32)G1_STAGE;
  };
  BAR();
  WAIT_L0();
#pragma unroll
  for (int m = 0; m < 15; ++m) rd(m, W0, H0);
  sv = *(LAS u32*)((LAS char*)(size_t)sa);
  aW += G1_STAGE; sa += G1_STAGE;
#pragma unroll
  for (int t = 0; t < 4; ++t) aH[t] += G1_STAGE;
#pragma unroll 1
  for (int ks = 0; ks < G1_NK; ks += 2) {
    WAIT_L0();
    BAR();
    step(W0, H0, W1, H1, advance(ks), sv, svn);
    WAIT_L0();
    BAR();
    step(W1, H1, W0, H0, advance(ks + 1), svn, sv);
  }
  LAS float* st = (LAS float*)(sm + G1_ST);
  if (lane < 16) {
#pragma unroll
    for (int t = 0; t < 2; ++t) st[(4 * mh + ((t + 2 * nh) & 3)) * 16 + lane] = rbf(accS[t][0] * (1.f / D_));
  }
  __syncthreads();
  float mean[4];
#pragma unroll
  for (int t = 0; t < 4; ++t) mean[t] = st[(4 * mh + ((t + 2 * nh) & 3)) * 16 + (lane & 15)];
  const int g0 = ((27 * fs) >> 1) + 7 * nh;
  const srd_t so = mk_srd(Hp, 32 * 54 * 4096);
  f32x4 cv[7];
#pragma unroll
  for (int j = 0; j < 7; ++j) cv[j] = *(LAS f32x4*)(sm + G1_CS + ((7 * nh + j) * 16 + 4 * q) * 4);
#pragma unroll
  for (int pr = 0; pr < 2; ++pr) {
    const int tt = 4 * mh + ((2 * pr + (q & 1) + 2 * nh) & 3);
    const u32 base = (u32)((2 * mt + (tt >> 2)) * 54 * 4096 + (tt & 3) * 1024 + (lane & 15) * 16);
    u32 pk[7][2];
#pragma unroll
    for (int j = 0; j < 7; ++j) {
      const f32x4 a = __builtin_elementwise_fma(cv[j], (f32x4)(-mean[2 * pr]), acc[2 * pr][j]);
      const f32x4 b = __builtin_elementwise_fma(cv[j], (f32x4)(-mean[2 * pr + 1]), acc[2 * pr + 1][j]);
      f32x4 gv, uv;
#pragma unroll
      for (int r = 0; r < 4; ++r) {
        auto s = __builtin_amdgcn_permlane16_swap(__float_as_uint(a[r]), __float_as_uint(b[r]), false, false);
        gv[r] = __uint_as_float(s[0]); uv[r] = __uint_as_float(s[1]);
      }
      const f32x4 pv = gv * uv;
      pk[j][0] = cvtpk(pv[0], pv[1]); pk[j][1] = cvtpk(pv[2], pv[3]);
      if (j & 1) {
        auto s0 = __builtin_amdgcn_permlane32_swap(pk[j - 1][0], pk[j][0], false, false);
        auto s1x = __builtin_amdgcn_permlane32_swap(pk[j - 1][1], pk[j][1], false, false);
        const int gi = g0 + j - 1 + (q >> 1);
        __builtin_amdgcn_raw_buffer_store_b128((u32x4){s0[0], s1x[0], s0[1], s1x[1]}, so, base + (gi >> 2) * 4096 + (gi & 3) * 256, 0, H_AUX);
      }
    }
    const int gi = g0 + 6;
    __builtin_amdgcn_raw_buffer_store_b64((u32x2){pk[6][0], pk[6][1]}, so, base + (gi >> 2) * 4096 + (gi & 3) * 256 + 8 * (q >> 1), 0, H_AUX);
  }
  {
    a1 = dpp_add<0x128>(a1); b1 = dpp_add<0x128>(b1); a2 = dpp_add<0x128>(a2); b2 = dpp_add<0x128>(b2);
    a1 = dpp_add<0x124>(a1); b1 = dpp_add<0x124>(b1); a2 = dpp_add<0x124>(a2); b2 = dpp_add<0x124>(b2);
    a1 = dpp_add<0x122>(a1); b1 = dpp_add<0x122>(b1); a2 = dpp_add<0x122>(a2); b2 = dpp_add<0x122>(b2);
    a1 = dpp_add<0x121>(a1); b1 = dpp_add<0x121>(b1); a2 = dpp_add<0x121>(a2); b2 = dpp_add<0x121>(b2);
    float s1 = (lane & 1) ? b1 : a1, s2 = (lane & 1) ? b2 : a2;
    { auto r = __builtin_amdgcn_permlane16_swap(__float_as_uint(s1), __float_as_uint(s1), false, false); s1 = __uint_as_float(r[0]) + __uint_as_float(r[1]); }
    { auto r = __builtin_amdgcn_permlane16_swap(__float_as_uint(s2), __float_as_uint(s2), false, false); s2 = __uint_as_float(r[0]) + __uint_as_float(r[1]); }
    const float mn = s1 * (1.f / D_);
    const float var = fmaxf(s2 * (1.f / D_) - mn * mn, 0.f);
    const float rs = 1.f / bfhi(ln_pack(mn, var));
    if (lane < 2) r2[m0 + 8 * fs + 2 * w + lane] = rs * rs;
  }
}

#define G2_RW 8
#define G2_RH 8
constexpr int G2_NW = G2_RW, G2_NH = G2_RH, G2_NK = 27;
constexpr int G2_H = G2_NW * 10240, G2_RES = G2_H + G2_NH * 8192, G2_LDS = G2_RES + 10240;
static_assert(G2_LDS <= 163840, "LDS");

constexpr int g2_end(int j, int N) {
  int e = 0;
  for (int k = -1; k <= j; ++k) {
    const int per = k < 0 ? 4 : (k == 0 ? 1 : (k == 1 ? 4 : 3)), lim = k <= 0 ? N : 2 * k - 1 + N;
    e = e + per < lim ? e + per : lim;
    e = e < G2_NK ? e : G2_NK;
  }
  return e;
}
constexpr int g2_first(int j, int N) { return j < 0 ? 0 : g2_end(j - 1, N); }
constexpr int G2_RESJ = 6;
constexpr int g2_younger(int tile, int last, int P, int N) {
  int n = 0;
  bool seen = false;
  for (int j = -1; j <= last; ++j) {
    if (seen && j == G2_RESJ) n += 3;
    for (int t = g2_first(j, N); t < g2_end(j, N); ++t) { if (seen) n += P; if (t == tile) seen = true; }
  }
  return n < 63 ? n : 63;
}

template <bool ISW>
DI void g2_loader(const char* __restrict__ Hp, const char* __restrict__ wp2, const u16* __restrict__ hT, LAS char* sm, const int lw, const int lane,
                  const int mt, const int nt) {
  constexpr int P = ISW ? 5 : 4, N = ISW ? G2_NW : G2_NH, TB = ISW ? 10240 : 8192;
  const u32 lds0 = (u32)(size_t)sm;
  const srd_t srd = ISW ? mk_srd(wp2 + (size_t)nt * 27 * 10240, 27 * 10240) : mk_srd(Hp + (size_t)mt * 54 * 4096, 54 * 4096);
  const u32 vo = lane * 16;
  const u32 l0 = lds0 + (ISW ? 5120 * lw : G2_H + 4096 * (lw - 2));
  u32 lb = l0, so = ISW ? 5120 * lw : 4096 * (lw - 2);
  auto tile = [&]() {
#pragma unroll
    for (int i = 0; i < P; ++i) dma16(lb + 1024 * i, vo, srd, so + 1024 * i);
    so += TB;
    lb = lb == l0 + (N - 1) * TB ? l0 : lb + TB;
  };
#pragma unroll
  for (int t = g2_first(-1, N); t < g2_end(-1, N); ++t) tile();
  wait_vm<g2_younger(0, -1, P, N)>();
  BAR();
  auto interval = [&](auto jc) {
    constexpr int j = decltype(jc)::value;
    if (j == G2_RESJ) {
      const srd_t srdR = mk_srd(hT + (size_t)nt * 80 * L_ + mt * 64, (u32)((D_ - nt * 80) * L_ - mt * 64) * 2u);
#pragma unroll
      for (int i = 0; i < 3; ++i) {
        const int pc = min(lw + 4 * i, 9);
        dma16(lds0 + G2_RES + 1024 * pc, (u32)((8 * pc + (lane >> 3)) * (L_ * 2) + (lane & 7) * 16), srdR, 0u);
      }
    }
#pragma unroll
    for (int t = g2_first(j, N); t < g2_end(j, N); ++t) tile();
    constexpr int need = 2 * j + 2 < G2_NK ? 2 * j + 2 : G2_NK - 1;
    if (j >= 12) wait_vm<0>(); else wait_vm<g2_younger(need, j, P, N)>();
    BAR();
  };
  static_for<14>(interval);
  __syncthreads();
}

extern "C" __global__ __launch_bounds__(512, 2) void s4_gemm2(const char* __restrict__ Hp, const char* __restrict__ wp2, const u16* __restrict__ hT,
                                                const float* __restrict__ r2, u16* __restrict__ out) {
  extern __shared__ __attribute__((aligned(16))) char smem[];
  LAS char* sm = (LAS char*)smem;
  const u32 lds0 = (u32)(size_t)sm;
  const int tid = threadIdx.x, lane = tid & 63;
  const int w = rfl(tid >> 6);
  const int bid = blockIdx.x, xcd = bid & 7, jj = bid >> 3;
  const int fg = xcd >> 1;
  const int mt = (xcd & 1) * 16 + (fg >> 1) * 8 + (jj & 7), nt = (fg & 1) * 4 + (jj >> 3);
  const int m0 = mt * 64, n0 = nt * 80;

  if (w >= 4) {
    if (w < 6) g2_loader<true>(Hp, wp2, hT, sm, w - 4, lane, mt, nt);
    else g2_loader<false>(Hp, wp2, hT, sm, w - 4, lane, mt, nt);
    return;
  }

  const int th = w >> 1, kh = w & 1;
  u32 aW = lds0 + kh * 5120 + lane * 16, aH = lds0 + G2_H + kh * 4096 + th * 2048 + lane * 16;
  f32x4 acc[2][5];
#pragma unroll
  for (int t = 0; t < 2; ++t)
#pragma unroll
    for (int d = 0; d < 5; ++d) acc[t][d] = (f32x4){0.f, 0.f, 0.f, 0.f};
  const float rsc = r2[m0 + 16 * (2 * th + kh) + (lane & 15)];
  bf16x8 W0[5], W1[5], H0[2], H1[2];
  auto rd = [&](int m, bf16x8 (&W)[5], bf16x8 (&H)[2]) {
    if (m < 2) H[m] = *(LAS bf16x8*)((LAS char*)(size_t)aH + m * 1024);
    else W[m - 2] = *(LAS bf16x8*)((LAS char*)(size_t)aW + (m - 2) * 1024);
  };
  int rw = 0, rh = 0;
  auto nextW = [&]() { const u32 adv = rw == G2_NW - 1 ? (u32)(-(G2_NW - 1) * 10240) : 10240u; rw = rw == G2_NW - 1 ? 0 : rw + 1; return adv; };
  auto nextH = [&]() { const u32 adv = rh == G2_NH - 1 ? (u32)(-(G2_NH - 1) * 8192) : 8192u; rh = rh == G2_NH - 1 ? 0 : rh + 1; return adv; };
  auto step = [&](const bf16x8 (&W)[5], const bf16x8 (&H)[2], bf16x8 (&Wn)[5], bf16x8 (&Hn)[2]) {
    const u32 advW = nextW(), advH = nextH();
#pragma unroll
    for (int m = 0; m < 10; ++m) {
      acc[m & 1][m >> 1] = mfma16(W[m >> 1], H[m & 1], acc[m & 1][m >> 1]);
      if (m < 7) rd(m, Wn, Hn);
      if (m == 7) vadd_s(aW, advW);
      if (m == 8) vadd_s(aH, advH);
      PIN();
    }
  };
  BAR();
  WAIT_L0();
  {
#pragma unroll
    for (int m = 0; m < 7; ++m) rd(m, W0, H0);
    aW += nextW(); aH += nextH();
  }
#pragma unroll 1
  for (int j = 0; j < 13; ++j) {
    WAIT_L0();
    BAR();
    step(W0, H0, W1, H1);
    WAIT_L0();
    step(W1, H1, W0, H0);
  }
  WAIT_L0();
  BAR();
#pragma unroll
  for (int m = 0; m < 10; ++m) acc[m & 1][m >> 1] = mfma16(W0[m >> 1], H0[m & 1], acc[m & 1][m >> 1]);
  {
    LAS char* ex = sm + w * 5120 + lane * 16;
#pragma unroll
    for (int d = 0; d < 5; ++d) *(LAS f32x4*)(ex + d * 1024) = kh ? acc[0][d] : acc[1][d];
  }
  __syncthreads();
  const int q = lane >> 4, qq = (lane & 15) >> 2, p = lane & 3;
  const int tt = 2 * th + kh;
  const srd_t so = mk_srd(out, L_ * D_ * 2);
  const u32 oo = (u32)((m0 + 16 * tt + (lane & 15)) * D_ + n0 + 4 * q) * 2u;
#pragma unroll
  for (int d = 0; d < 5; ++d) {
    const f32x4 o = ((kh ? acc[1][d] : acc[0][d]) + *(LAS f32x4*)(sm + (w ^ 1) * 5120 + lane * 16 + d * 1024)) * rsc;
    const u32x2 hr = __builtin_bit_cast(u32x2, trread(sm + G2_RES + (16 * d + 4 * q + qq) * 128 + 32 * tt + 8 * p));
    const float o0 = bflo(hr[0]) + o[0], o1 = bfhi(hr[0]) + o[1], o2 = bflo(hr[1]) + o[2], o3 = bfhi(hr[1]) + o[3];
    __builtin_amdgcn_raw_buffer_store_b64((u32x2){cvtpk(o0, o1), cvtpk(o2, o3)}, so, oo + 32 * d, 0, OUT_AUX);
  }
}

constexpr size_t WS_EPOCH = 0, WS_GRAN = 4096, WS_R2 = WS_GRAN + 16384, WS_CS = WS_R2 + 8192, WS_HT = 262144, WS_H = WS_HT + (size_t)D_ * L_ * 2,
                 WS_WP1 = WS_H + (size_t)32 * 54 * 4096, WS_WP2 = WS_WP1 + (size_t)16 * 10 * 28672;

extern "C" void kernel_launch(void* const* d_in, const int* in_sizes, int n_in, void* d_out, int out_size, void* d_ws, size_t ws_size,
                              hipStream_t stream) {
  char* ws = (char*)d_ws;
  const u16* x = (const u16*)d_in[0];
  u16* hT = (u16*)(ws + WS_HT);
  float* csp = (float*)(ws + WS_CS);
  (void)hipFuncSetAttribute((const void*)s4_k12, hipFuncAttributeMaxDynamicSharedMemorySize, K12_LDS);
  (void)hipFuncSetAttribute((const void*)s4_gemm1, hipFuncAttributeMaxDynamicSharedMemorySize, G1_LDS);
  (void)hipFuncSetAttribute((const void*)s4_gemm2, hipFuncAttributeMaxDynamicSharedMemorySize, G2_LDS);
  s4_k12<<<256, 512, K12_LDS, stream>>>(x, (const u16*)d_in[1], (const u16*)d_in[2], (const u16*)d_in[3], (const u16*)d_in[4],
                                     (const u16*)d_in[5], (const u16*)d_in[6], (const u16*)d_in[7], (const u16*)d_in[8],
                                     (const u16*)d_in[10], (const u16*)d_in[11], (const u16*)d_in[12], hT, ws + WS_WP1, ws + WS_WP2, csp,
                                     (u64*)(ws + WS_GRAN), (u32*)(ws + WS_EPOCH));
  s4_gemm1<<<256, 512, G1_LDS, stream>>>(hT, ws + WS_WP1, ws + WS_WP2, csp, ws + WS_H, (float*)(ws + WS_R2));
  s4_gemm2<<<256, 512, G2_LDS, stream>>>(ws + WS_H, ws + WS_WP2, hT, (const float*)(ws + WS_R2), (u16*)d_out);
}
```
